```python
import torch

def _softmax(x, axis=-1):
    return torch.softmax(x, dim=axis)
def _gelu(x):
    return torch.nn.functional.gelu(x, approximate="tanh")
def _silu(x):
    return torch.nn.functional.silu(x)
def _sigmoid(x):
    return torch.sigmoid(x)
def _layernorm(x, axis=-1, eps=1e-5):
    m = x.mean(dim=axis, keepdim=True)
    v = x.var(dim=axis, keepdim=True, unbiased=False)
    return (x - m) / torch.sqrt(v + eps)
def _scatter_add(tgt, idx, src, axis=0):
    return tgt.clone().index_add_(axis, idx, src)
def _segment_sum(src, seg, n):
    out = torch.zeros((n,) + src.shape[1:], dtype=src.dtype)
    return out.index_add_(0, seg, src)
def _pad(x, pw):
    flat = tuple(v for pair in reversed(pw) for v in pair)
    return torch.nn.functional.pad(x, flat)

kernel_name = 'concat_heads__grouped_kv_mqa__sliding_local_dilated__prompt'

def setup_inputs():

    _w = (lambda a: (lambda t: t.to(torch.bfloat16) if t.is_floating_point() else t)(torch.as_tensor(a)))
    def _A_setup():
        B, S, D, H = 1, 2048, 448, 16
        Dh = D // H
        return {
            "x": _w((lambda *s: torch.randn(*s, dtype=torch.bfloat16) * 0.05)(B, S, D)),
            "wq": _w((lambda *s: torch.randn(*s, dtype=torch.bfloat16) * 3.2)(D, H * Dh)),
            "wk": _w((lambda *s: torch.randn(*s, dtype=torch.bfloat16) * 3.2)(D, Dh)),
            "wv": _w((lambda *s: torch.randn(*s, dtype=torch.bfloat16) * 1.6)(D, Dh)),
            "wo": _w((lambda *s: torch.randn(*s, dtype=torch.bfloat16) * 1.6)(H * Dh, D)),
        }
    _A_in = _A_setup()

    def _B_setup():
        B, S, D = 1, 2048, 448
        return {
            "x": _w((lambda *s: torch.randn(*s, dtype=torch.bfloat16) * 0.1)(B, S, D)),
            "wqkv": _w((lambda *s: torch.randn(*s, dtype=torch.bfloat16) * 0.8)(D, 3 * D)),
            "wo": _w((lambda *s: torch.randn(*s, dtype=torch.bfloat16) * 0.8)(D, D)),
        }
    _B_in = _B_setup()

    return {
        "A_x": _A_in["x"],
        "A_wq": _A_in["wq"],
        "A_wk": _A_in["wk"],
        "A_wv": _A_in["wv"],
        "A_wo": _A_in["wo"],
        "B_x": _B_in["x"],
        "B_wqkv": _B_in["wqkv"],
        "B_wo": _B_in["wo"],
    }

def reference(A_x, A_wq, A_wk, A_wv, A_wo, B_x, B_wqkv, B_wo):
    def _A_ref(x, wq, wk, wv, wo):
        B, S, D = x.shape
        H = 16
        Dh = D // H
        q = (lambda x, *d: x.permute(*d))((x @ wq).reshape(B, S, H, Dh), 0, 2, 1, 3)
        k = x @ wk
        v = x @ wv
        scores = torch.einsum("bhqd,bkd->bhqk", q, k) * (Dh ** -0.5)
        p = _softmax(scores, axis=-1)
        o = torch.einsum("bhqk,bkd->bhqd", p, v)
        return (lambda x, *d: x.permute(*d))(o, 0, 2, 1, 3).reshape(B, S, H * Dh) @ wo

    def _B_ref(x, wqkv, wo):
        B, S, D = x.shape
        H = 16; Dh = D // H
        W, dil = 8, 2
        qkv = (lambda x, *d: x.permute(*d))((x @ wqkv).reshape(B, S, 3, H, Dh), 2, 0, 3, 1, 4)
        q, k, v = qkv[0], qkv[1], qkv[2]
        scale = Dh ** -0.5
        pos = torch.arange(S)
        off = torch.arange(W) * dil
        idx = pos[:, None] - off[None, :]
        mask = idx >= 0
        idx_c = torch.where(mask, idx, 0).reshape(-1)
        kg = k[:, :, idx_c, :].reshape(B, H, S, W, Dh)
        vg = v[:, :, idx_c, :].reshape(B, H, S, W, Dh)
        logits = torch.einsum("bhsd,bhswd->bhsw", q, kg) * scale
        logits = torch.where(mask[None, None, :, :], logits, -1e9)
        attn = _softmax(logits, axis=-1)
        o = torch.einsum("bhsw,bhswd->bhsd", attn, vg)
        return (lambda x, *d: x.permute(*d))(o, 0, 2, 1, 3).reshape(B, S, D) @ wo

    a = _A_ref(A_x, A_wq, A_wk, A_wv, A_wo)
    b = _B_ref(B_x, B_wqkv, B_wo)
    return torch.concatenate([a, b], axis=-1)
```

```cpp
#include <hip/hip_runtime.h>
#include <hip/hip_bf16.h>
#include <stdint.h>

constexpr int S_ = 2048, D_ = 448, H_ = 16, DH = 28;
constexpr int KDIM = 448;
constexpr float SCALE_F = 0.18898223650461362f;
constexpr float LOG2E = 1.4426950408889634f;

typedef unsigned short u16;
typedef short bf16x8 __attribute__((ext_vector_type(8)));
typedef short s16x4 __attribute__((ext_vector_type(4)));
typedef float f32x16 __attribute__((ext_vector_type(16)));
typedef float f32x2 __attribute__((ext_vector_type(2)));
typedef __bf16 bf16x2_t __attribute__((ext_vector_type(2)));
typedef unsigned u32x4 __attribute__((ext_vector_type(4)));
typedef unsigned u32x2 __attribute__((ext_vector_type(2)));

#define DI __device__ __forceinline__

DI unsigned pack_bf16(float lo, float hi) {
  f32x2 v = {lo, hi};
  bf16x2_t b = __builtin_convertvector(v, bf16x2_t);
  return __builtin_bit_cast(unsigned, b);
}
DI float rnd_bf16(float x) { return __uint_as_float(pack_bf16(0.f, x)); }
DI float bf_lo(unsigned u) { return __uint_as_float(u << 16); }
DI float bf_hi(unsigned u) { return __uint_as_float(u & 0xffff0000u); }

#define WAIT_VM(n) asm volatile("s_waitcnt vmcnt(" #n ")" ::: "memory")
template <int N> DI void wait_vm() { asm volatile("s_waitcnt vmcnt(%0)" ::"n"(N) : "memory"); }
DI void raw_barrier() {
  asm volatile("s_waitcnt lgkmcnt(0)" ::: "memory");
  __builtin_amdgcn_s_barrier();
}

DI void glds16(const void* g, void* l) {
  __builtin_amdgcn_global_load_lds((const unsigned*)g, (unsigned*)l, 16, 0, 0);
}
DI void glds4(const void* g, void* l) {
  __builtin_amdgcn_global_load_lds((const unsigned*)g, (unsigned*)l, 4, 0, 0);
}
DI s16x4 tr_read(const void* l) {
  return __builtin_amdgcn_ds_read_tr16_b64_v4i16((__attribute__((address_space(3))) s16x4*)l);
}

constexpr size_t WS_QA = 0;
constexpr size_t WS_KV = WS_QA + (size_t)S_ * D_ * 2;
constexpr size_t WS_QKVB = WS_KV + 64 * 4096;
constexpr size_t WS_OA = WS_QKVB + (size_t)S_ * 3 * D_ * 2;
constexpr size_t WS_OB = WS_OA + (size_t)S_ * D_ * 2;

template <int BM, int BN> struct GemmCfg {
  static constexpr int XB = BM * 128;
  static constexpr int WB = 64 * BN * 2;
  static constexpr int STB = XB + WB;
  static constexpr int NST = 4;
  static constexpr int XI = BM / 32;
  static constexpr int WI = BN / 32;
  static constexpr int MT = BM / 64, NT = BN / 64;
  static constexpr int CROW = BN * 2 + 16;
};

template <int BN> DI int wswz(int k) {
  if constexpr (BN == 128) return (k & 3) << 2;
  else return ((k >> 1) & 1) << 2;
}

template <int BM, int BN, bool KV>
DI void gemm_main(const u16* __restrict__ X,
                  const u16* __restrict__ W, int ldw, int n0, int ncols,
                  const u16* __restrict__ Wk, const u16* __restrict__ Wv,
                  char* smem, f32x16 (&acc)[BM / 64][BN / 64]) {
  using C = GemmCfg<BM, BN>;
  const int lane = threadIdx.x & 63;
  const int w = __builtin_amdgcn_readfirstlane(threadIdx.x >> 6);
  const int wm = w >> 1, wn = w & 1;
  constexpr int WCNT = KV ? 16 : C::WI;
  constexpr int CNT = C::XI + WCNT;

  const u16* xsrc[C::XI];
#pragma unroll
  for (int i = 0; i < C::XI; ++i) {
    int row = (w * C::XI + i) * 8 + (lane >> 3);
    int c = (lane & 7) ^ ((row >> 1) & 7);
    xsrc[i] = X + row * KDIM + c * 8;
  }
  const u16* wsrc[WCNT];
  if constexpr (!KV) {
    constexpr int CPR = BN / 8, RPI = 1024 / (BN * 2);
#pragma unroll
    for (int i = 0; i < C::WI; ++i) {
      int k = (w * C::WI + i) * RPI + lane / CPR;
      int c = (lane % CPR) ^ wswz<BN>(k);
      int col = n0 + c * 8;
      if (col + 8 > ncols) col = ncols - 8;
      wsrc[i] = W + k * ldw + col;
    }
  } else {
#pragma unroll
    for (int i = 0; i < 16; ++i) {
      int k = w * 16 + i;
      int c = (lane >> 2) ^ wswz<BN>(k);
      int col = c * 8 + (lane & 3) * 2;
      const u16* p = Wk + k * DH;
      if (col < 28) p = Wk + k * DH + col;
      else if (col >= 32 && col < 60) p = Wv + k * DH + (col - 32);
      wsrc[i] = p;
    }
  }
  auto issue = [&](int ks) {
    char* sb = smem + (ks % C::NST) * C::STB;
#pragma unroll
    for (int i = 0; i < C::XI; ++i) glds16(xsrc[i] + ks * 64, sb + (w * C::XI + i) * 1024);
    if constexpr (!KV) {
#pragma unroll
      for (int i = 0; i < C::WI; ++i) glds16(wsrc[i] + (size_t)ks * 64 * ldw, sb + C::XB + (w * C::WI + i) * 1024);
    } else {
#pragma unroll
      for (int i = 0; i < 16; ++i) glds4(wsrc[i] + ks * 64 * DH, sb + C::XB + (w * 16 + i) * 256);
    }
  };

  const int hh = lane >> 5;
  int xoff[C::MT];
#pragma unroll
  for (int i = 0; i < C::MT; ++i) {
    int row = wm * (BM / 2) + 32 * i + (lane & 31);
    xoff[i] = row * 128;
  }
  const int xsw = ((wm * (BM / 2) + (lane & 31)) >> 1) & 7;
  const int g = lane >> 4, q = (lane & 15) >> 2, p = lane & 3;
  int woff[C::NT];
#pragma unroll
  for (int j = 0; j < C::NT; ++j) {
    int nn = wn * (BN / 2) + 32 * j + 16 * (g & 1);
    int c = (nn >> 3) + (p >> 1);
    int kk = 8 * (g >> 1) + q;
    woff[j] = C::XB + kk * (BN * 2) + ((c ^ wswz<BN>(kk)) << 4) + (p & 1) * 8;
  }

#pragma unroll
  for (int i = 0; i < C::MT; ++i)
#pragma unroll
    for (int j = 0; j < C::NT; ++j)
#pragma unroll
      for (int r = 0; r < 16; ++r) acc[i][j][r] = 0.f;

  issue(0); issue(1); issue(2);
#pragma unroll
  for (int ks = 0; ks < 7; ++ks) {
    if (ks <= 4) wait_vm<2 * CNT>();
    else if (ks == 5) wait_vm<CNT>();
    else wait_vm<0>();
    raw_barrier();
    if (ks + 3 <= 6) issue(ks + 3);
    const char* sb = smem + (ks % C::NST) * C::STB;
#pragma unroll
    for (int k16 = 0; k16 < 4; ++k16) {
      bf16x8 xa[C::MT], wb[C::NT];
#pragma unroll
      for (int i = 0; i < C::MT; ++i)
        xa[i] = *(const bf16x8*)(sb + xoff[i] + (((2 * k16 + hh) ^ xsw) << 4));
#pragma unroll
      for (int j = 0; j < C::NT; ++j) {
        s16x4 lo = tr_read(sb + woff[j] + (16 * k16) * (BN * 2));
        s16x4 hi = tr_read(sb + woff[j] + (16 * k16 + 4) * (BN * 2));
        wb[j] = __builtin_shufflevector(lo, hi, 0, 1, 2, 3, 4, 5, 6, 7);
      }
#pragma unroll
      for (int i = 0; i < C::MT; ++i)
#pragma unroll
        for (int j = 0; j < C::NT; ++j)
          acc[i][j] = __builtin_amdgcn_mfma_f32_32x32x16_bf16(wb[j], xa[i], acc[i][j], 0, 0, 0);
    }
  }
  raw_barrier();
#pragma unroll
  for (int i = 0; i < C::MT; ++i)
#pragma unroll
    for (int j = 0; j < C::NT; ++j)
#pragma unroll
      for (int gq = 0; gq < 4; ++gq) {
        int pos = wm * (BM / 2) + 32 * i + (lane & 31);
        int n = wn * (BN / 2) + 32 * j + 8 * gq + 4 * hh;
        u32x2 v = {pack_bf16(acc[i][j][4 * gq], acc[i][j][4 * gq + 1]),
                   pack_bf16(acc[i][j][4 * gq + 2], acc[i][j][4 * gq + 3])};
        *(u32x2*)(smem + pos * C::CROW + n * 2) = v;
      }
  raw_barrier();
}

template <int BM, int BN>
DI void store_tile(const char* smem, u16* __restrict__ dst, int ldd, int nvalid) {
  using C = GemmCfg<BM, BN>;
  constexpr int CPR = BN / 8;
  constexpr int TOT = BM * CPR;
#pragma unroll
  for (int it = 0; it < TOT / 256; ++it) {
    int id = it * 256 + threadIdx.x;
    int row = id / CPR, c = id % CPR;
    u32x4 v = *(const u32x4*)(smem + row * C::CROW + c * 16);
    if (c * 8 < nvalid) *(u32x4*)(dst + (size_t)row * ldd + c * 8) = v;
  }
}

__global__ __launch_bounds__(256, 1) void proj_kernel(
    const u16* __restrict__ xA, const u16* __restrict__ wq, const u16* __restrict__ wk,
    const u16* __restrict__ wv, const u16* __restrict__ xB, const u16* __restrict__ wqkv,
    char* __restrict__ ws) {
  constexpr int BM = 128, BN = 128;
  using C = GemmCfg<BM, BN>;
  __shared__ __attribute__((aligned(16))) char smem[C::NST * C::STB];
  const int mt = blockIdx.x & 15, nt = blockIdx.x >> 4;
  const int m0 = mt * BM;
  f32x16 acc[2][2];
  if (nt < 4) {
    gemm_main<BM, BN, false>(xA + (size_t)m0 * KDIM, wq, D_, nt * BN, D_, nullptr, nullptr, smem, acc);
    store_tile<BM, BN>(smem, (u16*)(ws + WS_QA) + (size_t)m0 * D_ + nt * BN, D_, D_ - nt * BN);
  } else if (nt == 4) {
    gemm_main<BM, BN, true>(xA + (size_t)m0 * KDIM, nullptr, 0, 0, 0, wk, wv, smem, acc);
    char* kv = ws + WS_KV + (size_t)(m0 >> 5) * 4096;
    for (int it = 0; it < 2; ++it) {
      int id = it * 256 + threadIdx.x;
      int pos = id >> 2, c = id & 3;
      u32x4 v = *(const u32x4*)(smem + pos * C::CROW + c * 16);
      if (c == 3) { v[2] = 0; v[3] = 0; }
      int t = pos >> 5, s = c >> 1, hh = c & 1;
      *(u32x4*)(kv + t * 4096 + s * 1024 + ((pos & 31) + 32 * hh) * 16) = v;
    }
    for (int it = 0; it < 2; ++it) {
      int id = it * 256 + threadIdx.x;
      int t = id >> 7, s = (id >> 6) & 1, ln = id & 63;
      int d = ln & 31, hh = ln >> 5;
      u16 e[8];
#pragma unroll
      for (int j = 0; j < 8; ++j) {
        int key = 32 * t + 16 * s + 8 * (j >> 2) + 4 * hh + (j & 3);
        u16 val = *(const u16*)(smem + key * C::CROW + (32 + d) * 2);
        if (d == 28) val = 0x3F80;
        else if (d > 28) val = 0;
        e[j] = val;
      }
      u32x4 v = {(unsigned)e[0] | ((unsigned)e[1] << 16), (unsigned)e[2] | ((unsigned)e[3] << 16),
                 (unsigned)e[4] | ((unsigned)e[5] << 16), (unsigned)e[6] | ((unsigned)e[7] << 16)};
      *(u32x4*)(kv + t * 4096 + 2048 + s * 1024 + ln * 16) = v;
    }
  } else {
    const int n0 = (nt - 5) * BN;
    gemm_main<BM, BN, false>(xB + (size_t)m0 * KDIM, wqkv, 3 * D_, n0, 3 * D_, nullptr, nullptr, smem, acc);
    store_tile<BM, BN>(smem, (u16*)(ws + WS_QKVB) + (size_t)m0 * 3 * D_ + n0, 3 * D_, 3 * D_ - n0);
  }
}

__global__ __launch_bounds__(256, 1) void outproj_kernel(
    const char* __restrict__ ws, const u16* __restrict__ woA, const u16* __restrict__ woB,
    u16* __restrict__ out) {
  constexpr int BM = 128, BN = 64;
  using C = GemmCfg<BM, BN>;
  __shared__ __attribute__((aligned(16))) char smem[C::NST * C::STB];
  const int mt = blockIdx.x & 15, r = blockIdx.x >> 4;
  const int br = r >= 7, nt = br ? r - 7 : r;
  const int m0 = mt * BM;
  f32x16 acc[2][1];
  const u16* X = (const u16*)(ws + (br ? WS_OB : WS_OA)) + (size_t)m0 * KDIM;
  gemm_main<BM, BN, false>(X, br ? woB : woA, D_, nt * BN, D_, nullptr, nullptr, smem, acc);
  store_tile<BM, BN>(smem, out + (size_t)m0 * (2 * D_) + br * D_ + nt * BN, 2 * D_, BN);
}

DI float dpp_xor1(float x) {
  return __uint_as_float(__builtin_amdgcn_update_dpp(0, __float_as_uint(x), 0xB1, 0xf, 0xf, true));
}
DI float dpp_xor2(float x) {
  return __uint_as_float(__builtin_amdgcn_update_dpp(0, __float_as_uint(x), 0x4E, 0xf, 0xf, true));
}

DI void load28(const u16* p, float (&f)[28]) {
#pragma unroll
  for (int i = 0; i < 7; ++i) {
    u32x2 v = *(const u32x2*)(p + 4 * i);
    f[4 * i] = bf_lo(v[0]); f[4 * i + 1] = bf_hi(v[0]);
    f[4 * i + 2] = bf_lo(v[1]); f[4 * i + 3] = bf_hi(v[1]);
  }
}

DI void local_attn_item(const u16* __restrict__ qkv, u16* __restrict__ oB, int item, int sub) {
  const int pos = item >> 4, h = item & 15;
  float q[28], ka[28], kb[28], va[28], vb[28];
  const int pa = pos - 2 * (2 * sub), pb = pos - 2 * (2 * sub + 1);
  const int pac = pa < 0 ? 0 : pa, pbc = pb < 0 ? 0 : pb;
  load28(qkv + (size_t)pos * 1344 + h * DH, q);
  load28(qkv + (size_t)pac * 1344 + 448 + h * DH, ka);
  load28(qkv + (size_t)pbc * 1344 + 448 + h * DH, kb);
  load28(qkv + (size_t)pac * 1344 + 896 + h * DH, va);
  load28(qkv + (size_t)pbc * 1344 + 896 + h * DH, vb);
  float sa = 0.f, sb = 0.f;
#pragma unroll
  for (int d = 0; d < 28; ++d) { sa = fmaf(q[d], ka[d], sa); sb = fmaf(q[d], kb[d], sb); }
  float la = rnd_bf16(rnd_bf16(sa) * SCALE_F), lb = rnd_bf16(rnd_bf16(sb) * SCALE_F);
  if (pa < 0) la = -1e9f;
  if (pb < 0) lb = -1e9f;
  float m = fmaxf(la, lb);
  m = fmaxf(m, dpp_xor1(m));
  m = fmaxf(m, dpp_xor2(m));
  float ea = __builtin_amdgcn_exp2f((la - m) * LOG2E), eb = __builtin_amdgcn_exp2f((lb - m) * LOG2E);
  float sum = ea + eb;
  sum += dpp_xor1(sum);
  sum += dpp_xor2(sum);
  float inv = 1.0f / sum;
  float pA = rnd_bf16(ea * inv), pB = rnd_bf16(eb * inv);
  float o[28];
#pragma unroll
  for (int d = 0; d < 28; ++d) {
    float t = pA * va[d] + pB * vb[d];
    t += dpp_xor1(t);
    t += dpp_xor2(t);
    o[d] = t;
  }
  u16* dst = oB + (size_t)pos * D_ + h * DH + 8 * sub;
  float w0, w1, w2, w3, w4, w5, w6, w7;
  if (sub == 0) { w0 = o[0]; w1 = o[1]; w2 = o[2]; w3 = o[3]; w4 = o[4]; w5 = o[5]; w6 = o[6]; w7 = o[7]; }
  else if (sub == 1) { w0 = o[8]; w1 = o[9]; w2 = o[10]; w3 = o[11]; w4 = o[12]; w5 = o[13]; w6 = o[14]; w7 = o[15]; }
  else if (sub == 2) { w0 = o[16]; w1 = o[17]; w2 = o[18]; w3 = o[19]; w4 = o[20]; w5 = o[21]; w6 = o[22]; w7 = o[23]; }
  else { w0 = o[24]; w1 = o[25]; w2 = o[26]; w3 = o[27]; w4 = 0; w5 = 0; w6 = 0; w7 = 0; }
  u32x2 v0 = {pack_bf16(w0, w1), pack_bf16(w2, w3)};
  *(u32x2*)dst = v0;
  if (sub < 3) {
    u32x2 v1 = {pack_bf16(w4, w5), pack_bf16(w6, w7)};
    *(u32x2*)(dst + 4) = v1;
  }
}

constexpr float THR = 8.0f;

__global__ __launch_bounds__(512, 2) void attn_kernel(char* __restrict__ ws) {
  __shared__ __attribute__((aligned(16))) char smem[3 * 32768];
  const int lane = threadIdx.x & 63;
  const int w = __builtin_amdgcn_readfirstlane(threadIdx.x >> 6);
  const int qb = w & 3, hf = w >> 2;
  const int head = blockIdx.x & 15, pb = blockIdx.x >> 4;
  const int hh = lane >> 5;
  const char* kvg = ws + WS_KV;

  auto issue = [&](int st) {
    const char* src = kvg + (size_t)(hf * 32 + st * 4) * 4096 + (qb * 4) * 1024 + lane * 16;
    char* dst = smem + (st % 3) * 32768 + hf * 16384 + (qb * 4) * 1024;
#pragma unroll
    for (int i = 0; i < 4; ++i) glds16(src + i * 1024, dst + i * 1024);
  };
  issue(0); issue(1);

  const int pos = pb * 128 + qb * 32 + (lane & 31);
  bf16x8 q0, q1;
  {
    const u16* qp = (const u16*)(ws + WS_QA) + (size_t)pos * D_ + head * DH + 8 * hh;
    u32x2 a = *(const u32x2*)(qp), b = *(const u32x2*)(qp + 4);
    u32x2 c = *(const u32x2*)(qp + 16);
    u32x2 d = {0u, 0u};
    if (hh == 0) d = *(const u32x2*)(qp + 20);
    u32x4 x0 = {a[0], a[1], b[0], b[1]}, x1 = {c[0], c[1], d[0], d[1]};
    q0 = __builtin_bit_cast(bf16x8, x0);
    q1 = __builtin_bit_cast(bf16x8, x1);
  }

  local_attn_item((const u16*)(ws + WS_QKVB), (u16*)(ws + WS_OB), blockIdx.x * 128 + (threadIdx.x >> 2), threadIdx.x & 3);

  f32x16 O;
#pragma unroll
  for (int r = 0; r < 16; ++r) O[r] = 0.f;
  float R = -1e30f;

  for (int st = 0; st < 8; ++st) {
    if (st < 7) WAIT_VM(4); else WAIT_VM(0);
    raw_barrier();
    if (st + 2 < 8) issue(st + 2);
    const char* base = smem + (st % 3) * 32768 + hf * 16384 + lane * 16;
#pragma unroll
    for (int t = 0; t < 4; ++t) {
      bf16x8 k0 = *(const bf16x8*)(base + t * 4096);
      bf16x8 k1 = *(const bf16x8*)(base + t * 4096 + 1024);
      bf16x8 v0 = *(const bf16x8*)(base + t * 4096 + 2048);
      bf16x8 v1 = *(const bf16x8*)(base + t * 4096 + 3072);
      f32x16 s;
#pragma unroll
      for (int r = 0; r < 16; ++r) s[r] = 0.f;
      s = __builtin_amdgcn_mfma_f32_32x32x16_bf16(k0, q0, s, 0, 0, 0);
      s = __builtin_amdgcn_mfma_f32_32x32x16_bf16(k1, q1, s, 0, 0, 0);
      float b[16];
#pragma unroll
      for (int r = 0; r < 16; ++r) b[r] = s[r] * SCALE_F;
      float tm = fmaxf(b[0], b[1]);
#pragma unroll
      for (int r = 2; r < 16; r += 2) tm = fmaxf(tm, fmaxf(b[r], b[r + 1]));
      if (__builtin_expect(__any(tm > R + THR), 0)) {
        auto rr = __builtin_amdgcn_permlane32_swap(__float_as_uint(tm), __float_as_uint(tm), false, false);
        float full = fmaxf(__uint_as_float(rr[0]), __uint_as_float(rr[1]));
        float Rn = fmaxf(R, full);
        float alpha = __builtin_amdgcn_exp2f((R - Rn) * LOG2E);
#pragma unroll
        for (int r = 0; r < 16; ++r) O[r] *= alpha;
        R = Rn;
      }
      const float nR = -R * LOG2E;
      float p[16];
#pragma unroll
      for (int r = 0; r < 16; ++r) p[r] = __builtin_amdgcn_exp2f(fmaf(b[r], LOG2E, nR));
      u32x4 pa = {pack_bf16(p[0], p[1]), pack_bf16(p[2], p[3]), pack_bf16(p[4], p[5]), pack_bf16(p[6], p[7])};
      u32x4 pbv = {pack_bf16(p[8], p[9]), pack_bf16(p[10], p[11]), pack_bf16(p[12], p[13]), pack_bf16(p[14], p[15])};
      O = __builtin_amdgcn_mfma_f32_32x32x16_bf16(v0, __builtin_bit_cast(bf16x8, pa), O, 0, 0, 0);
      O = __builtin_amdgcn_mfma_f32_32x32x16_bf16(v1, __builtin_bit_cast(bf16x8, pbv), O, 0, 0, 0);
    }
  }

  raw_barrier();
  float* xch = (float*)smem + qb * (17 * 64);
  if (hf == 1) {
#pragma unroll
    for (int r = 0; r < 16; ++r) xch[r * 64 + lane] = O[r];
    xch[16 * 64 + lane] = R;
  }
  raw_barrier();
  if (hf == 0) {
    float R1 = xch[16 * 64 + lane];
    float Rn = fmaxf(R, R1);
    float a0 = __builtin_amdgcn_exp2f((R - Rn) * LOG2E), a1 = __builtin_amdgcn_exp2f((R1 - Rn) * LOG2E);
#pragma unroll
    for (int r = 0; r < 16; ++r) O[r] = O[r] * a0 + xch[r * 64 + lane] * a1;
    float l = __shfl(O[12], (lane & 31) + 32);
    float inv = 1.0f / l;
    u16* dst = (u16*)(ws + WS_OA) + (size_t)pos * D_ + head * DH + 4 * hh;
#pragma unroll
    for (int gq = 0; gq < 4; ++gq) {
      if (gq < 3 || hh == 0) {
        u32x2 v = {pack_bf16(O[4 * gq] * inv, O[4 * gq + 1] * inv), pack_bf16(O[4 * gq + 2] * inv, O[4 * gq + 3] * inv)};
        *(u32x2*)(dst + 8 * gq) = v;
      }
    }
  }
}

extern "C" void kernel_launch(void* const* d_in, const int* in_sizes, int n_in,
                              void* d_out, int out_size, void* d_ws, size_t ws_size,
                              hipStream_t stream) {
  const u16* xA = (const u16*)d_in[0];
  const u16* wq = (const u16*)d_in[1];
  const u16* wk = (const u16*)d_in[2];
  const u16* wv = (const u16*)d_in[3];
  const u16* woA = (const u16*)d_in[4];
  const u16* xB = (const u16*)d_in[5];
  const u16* wqkv = (const u16*)d_in[6];
  const u16* woB = (const u16*)d_in[7];
  char* ws = (char*)d_ws;
  proj_kernel<<<256, 256, 0, stream>>>(xA, wq, wk, wv, xB, wqkv, ws);
  attn_kernel<<<256, 512, 0, stream>>>(ws);
  outproj_kernel<<<224, 256, 0, stream>>>(ws, woA, woB, (u16*)d_out);
}
```

```cpp
#include <hip/hip_runtime.h>
#include <hip/hip_bf16.h>
#include <stdint.h>

constexpr int S_ = 2048, D_ = 448, H_ = 16, DH = 28;
constexpr int KDIM = 448;
constexpr float SCALE_F = 0.18898223650461362f;
constexpr float LOG2E = 1.4426950408889634f;

typedef unsigned short u16;
typedef short bf16x8 __attribute__((ext_vector_type(8)));
typedef short s16x4 __attribute__((ext_vector_type(4)));
typedef float f32x16 __attribute__((ext_vector_type(16)));
typedef float f32x2 __attribute__((ext_vector_type(2)));
typedef __bf16 bf16x2_t __attribute__((ext_vector_type(2)));
typedef unsigned u32x4 __attribute__((ext_vector_type(4)));
typedef unsigned u32x2 __attribute__((ext_vector_type(2)));

#define DI __device__ __forceinline__

DI unsigned pack_bf16(float lo, float hi) {
  f32x2 v = {lo, hi};
  bf16x2_t b = __builtin_convertvector(v, bf16x2_t);
  return __builtin_bit_cast(unsigned, b);
}
DI float rnd_bf16(float x) { return __uint_as_float(pack_bf16(0.f, x)); }
DI float bf_lo(unsigned u) { return __uint_as_float(u << 16); }
DI float bf_hi(unsigned u) { return __uint_as_float(u & 0xffff0000u); }

#define WAIT_VM(n) asm volatile("s_waitcnt vmcnt(" #n ")" ::: "memory")
template <int N> DI void wait_vm() { asm volatile("s_waitcnt vmcnt(%0)" ::"n"(N) : "memory"); }
DI void raw_barrier() {
  asm volatile("s_waitcnt lgkmcnt(0)" ::: "memory");
  __builtin_amdgcn_s_barrier();
}

DI void glds16(const void* g, void* l) {
  __builtin_amdgcn_global_load_lds((const unsigned*)g, (unsigned*)l, 16, 0, 0);
}
DI void glds4(const void* g, void* l) {
  __builtin_amdgcn_global_load_lds((const unsigned*)g, (unsigned*)l, 4, 0, 0);
}
DI s16x4 tr_read(const void* l) {
  return __builtin_amdgcn_ds_read_tr16_b64_v4i16((__attribute__((address_space(3))) s16x4*)l);
}

constexpr size_t WS_QA = 0;
constexpr size_t WS_KV = WS_QA + (size_t)S_ * D_ * 2;
constexpr size_t WS_QKVB = WS_KV + 64 * 4096;
constexpr size_t WS_OA = WS_QKVB + (size_t)S_ * 3 * D_ * 2;
constexpr size_t WS_OB = WS_OA + (size_t)S_ * D_ * 2;

constexpr int GNW = 8;
template <int BM, int BN> struct GemmCfg {
  static constexpr int XB = BM * 128;
  static constexpr int WB = 64 * BN * 2;
  static constexpr int STB = XB + WB;
  static constexpr int NST = 4;
  static constexpr int XI = XB / 1024 / GNW;
  static constexpr int WI = WB / 1024 / GNW;
  static constexpr int WGM = (BM >= BN) ? 4 : 2, WGN = GNW / WGM;
  static constexpr int MT = BM / 32 / WGM, NT = BN / 32 / WGN;
  static constexpr int CROW = BN * 2 + 16;
};

template <int BN> DI int wswz(int k) {
  if constexpr (BN == 128) return (k & 3) << 2;
  else return ((k >> 1) & 1) << 2;
}

template <int BM, int BN, bool KV>
DI void gemm_main(const u16* __restrict__ X,
                  const u16* __restrict__ W, int ldw, int n0, int ncols,
                  const u16* __restrict__ Wk, const u16* __restrict__ Wv,
                  char* smem) {
  using C = GemmCfg<BM, BN>;
  const int lane = threadIdx.x & 63;
  const int w = __builtin_amdgcn_readfirstlane(threadIdx.x >> 6);
  const int wm = w / C::WGN, wn = w % C::WGN;
  f32x16 acc[C::MT][C::NT];
  constexpr int KVI = 64 / GNW;
  constexpr int WCNT = KV ? KVI : C::WI;
  constexpr int CNT = C::XI + WCNT;

  const u16* xsrc[C::XI];
#pragma unroll
  for (int i = 0; i < C::XI; ++i) {
    int row = (w * C::XI + i) * 8 + (lane >> 3);
    int c = (lane & 7) ^ ((row >> 1) & 7);
    xsrc[i] = X + row * KDIM + c * 8;
  }
  const u16* wsrc[WCNT];
  if constexpr (!KV) {
    constexpr int CPR = BN / 8, RPI = 1024 / (BN * 2);
#pragma unroll
    for (int i = 0; i < C::WI; ++i) {
      int k = (w * C::WI + i) * RPI + lane / CPR;
      int c = (lane % CPR) ^ wswz<BN>(k);
      int col = n0 + c * 8;
      if (col + 8 > ncols) col = ncols - 8;
      wsrc[i] = W + k * ldw + col;
    }
  } else {
#pragma unroll
    for (int i = 0; i < KVI; ++i) {
      int k = w * KVI + i;
      int c = (lane >> 2) ^ wswz<BN>(k);
      int col = c * 8 + (lane & 3) * 2;
      const u16* p = Wk + k * DH;
      if (col < 28) p = Wk + k * DH + col;
      else if (col >= 32 && col < 60) p = Wv + k * DH + (col - 32);
      wsrc[i] = p;
    }
  }
  auto issue = [&](int ks) {
    char* sb = smem + (ks % C::NST) * C::STB;
#pragma unroll
    for (int i = 0; i < C::XI; ++i) glds16(xsrc[i] + ks * 64, sb + (w * C::XI + i) * 1024);
    if constexpr (!KV) {
#pragma unroll
      for (int i = 0; i < C::WI; ++i) glds16(wsrc[i] + (size_t)ks * 64 * ldw, sb + C::XB + (w * C::WI + i) * 1024);
    } else {
#pragma unroll
      for (int i = 0; i < KVI; ++i) glds4(wsrc[i] + ks * 64 * DH, sb + C::XB + (w * KVI + i) * 256);
    }
  };

  const int hh = lane >> 5;
  int xoff[C::MT];
#pragma unroll
  for (int i = 0; i < C::MT; ++i) {
    int row = wm * (BM / C::WGM) + 32 * i + (lane & 31);
    xoff[i] = row * 128;
  }
  const int xsw = ((lane & 31) >> 1) & 7;
  const int g = lane >> 4, q = (lane & 15) >> 2, p = lane & 3;
  int woff[C::NT];
#pragma unroll
  for (int j = 0; j < C::NT; ++j) {
    int nn = wn * (BN / C::WGN) + 32 * j + 16 * (g & 1);
    int c = (nn >> 3) + (p >> 1);
    int kk = 8 * (g >> 1) + q;
    woff[j] = C::XB + kk * (BN * 2) + ((c ^ wswz<BN>(kk)) << 4) + (p & 1) * 8;
  }

#pragma unroll
  for (int i = 0; i < C::MT; ++i)
#pragma unroll
    for (int j = 0; j < C::NT; ++j)
#pragma unroll
      for (int r = 0; r < 16; ++r) acc[i][j][r] = 0.f;

  issue(0); issue(1); issue(2);
#pragma unroll
  for (int ks = 0; ks < 7; ++ks) {
    if (ks <= 4) wait_vm<2 * CNT>();
    else if (ks == 5) wait_vm<CNT>();
    else wait_vm<0>();
    raw_barrier();
    if (ks + 3 <= 6) issue(ks + 3);
    const char* sb = smem + (ks % C::NST) * C::STB;
#pragma unroll
    for (int k16 = 0; k16 < 4; ++k16) {
      bf16x8 xa[C::MT], wb[C::NT];
#pragma unroll
      for (int i = 0; i < C::MT; ++i)
        xa[i] = *(const bf16x8*)(sb + xoff[i] + (((2 * k16 + hh) ^ xsw) << 4));
#pragma unroll
      for (int j = 0; j < C::NT; ++j) {
        s16x4 lo = tr_read(sb + woff[j] + (16 * k16) * (BN * 2));
        s16x4 hi = tr_read(sb + woff[j] + (16 * k16 + 4) * (BN * 2));
        wb[j] = __builtin_shufflevector(lo, hi, 0, 1, 2, 3, 4, 5, 6, 7);
      }
#pragma unroll
      for (int i = 0; i < C::MT; ++i)
#pragma unroll
        for (int j = 0; j < C::NT; ++j)
          acc[i][j] = __builtin_amdgcn_mfma_f32_32x32x16_bf16(wb[j], xa[i], acc[i][j], 0, 0, 0);
    }
  }
  raw_barrier();
#pragma unroll
  for (int i = 0; i < C::MT; ++i)
#pragma unroll
    for (int j = 0; j < C::NT; ++j)
#pragma unroll
      for (int gq = 0; gq < 4; ++gq) {
        int pos = wm * (BM / C::WGM) + 32 * i + (lane & 31);
        int n = wn * (BN / C::WGN) + 32 * j + 8 * gq + 4 * hh;
        u32x2 v = {pack_bf16(acc[i][j][4 * gq], acc[i][j][4 * gq + 1]),
                   pack_bf16(acc[i][j][4 * gq + 2], acc[i][j][4 * gq + 3])};
        *(u32x2*)(smem + pos * C::CROW + n * 2) = v;
      }
  raw_barrier();
}

template <int BM, int BN>
DI void store_tile(const char* smem, u16* __restrict__ dst, int ldd, int nvalid) {
  using C = GemmCfg<BM, BN>;
  constexpr int CPR = BN / 8;
  constexpr int TOT = BM * CPR;
#pragma unroll
  for (int it = 0; it < TOT / (64 * GNW); ++it) {
    int id = it * (64 * GNW) + threadIdx.x;
    int row = id / CPR, c = id % CPR;
    u32x4 v = *(const u32x4*)(smem + row * C::CROW + c * 16);
    if (c * 8 < nvalid) *(u32x4*)(dst + (size_t)row * ldd + c * 8) = v;
  }
}

__global__ __launch_bounds__(64 * GNW, 2) void proj_kernel(
    const u16* __restrict__ xA, const u16* __restrict__ wq, const u16* __restrict__ wk,
    const u16* __restrict__ wv, const u16* __restrict__ xB, const u16* __restrict__ wqkv,
    char* __restrict__ ws) {
  constexpr int BM = 128, BN = 128;
  using C = GemmCfg<BM, BN>;
  __shared__ __attribute__((aligned(16))) char smem[C::NST * C::STB];
  const int mt = blockIdx.x & 15, nt = blockIdx.x >> 4;
  const int m0 = mt * BM;
  if (nt < 4) {
    gemm_main<BM, BN, false>(xA + (size_t)m0 * KDIM, wq, D_, nt * BN, D_, nullptr, nullptr, smem);
    store_tile<BM, BN>(smem, (u16*)(ws + WS_QA) + (size_t)m0 * D_ + nt * BN, D_, D_ - nt * BN);
  } else if (nt == 4) {
    gemm_main<BM, BN, true>(xA + (size_t)m0 * KDIM, nullptr, 0, 0, 0, wk, wv, smem);
    char* kv = ws + WS_KV + (size_t)(m0 >> 5) * 4096;
    {
      int id = threadIdx.x;
      int pos = id >> 2, c = id & 3;
      u32x4 v = *(const u32x4*)(smem + pos * C::CROW + c * 16);
      if (c == 3) { v[2] = 0; v[3] = 0; }
      int t = pos >> 5, s = c >> 1, hh = c & 1;
      *(u32x4*)(kv + t * 4096 + s * 1024 + ((pos & 31) + 32 * hh) * 16) = v;
    }
    {
      int id = threadIdx.x;
      int t = id >> 7, s = (id >> 6) & 1, ln = id & 63;
      int d = ln & 31, hh = ln >> 5;
      u16 e[8];
#pragma unroll
      for (int j = 0; j < 8; ++j) {
        int key = 32 * t + 16 * s + 8 * (j >> 2) + 4 * hh + (j & 3);
        u16 val = *(const u16*)(smem + key * C::CROW + (32 + d) * 2);
        if (d == 28) val = 0x3F80;
        else if (d > 28) val = 0;
        e[j] = val;
      }
      u32x4 v = {(unsigned)e[0] | ((unsigned)e[1] << 16), (unsigned)e[2] | ((unsigned)e[3] << 16),
                 (unsigned)e[4] | ((unsigned)e[5] << 16), (unsigned)e[6] | ((unsigned)e[7] << 16)};
      *(u32x4*)(kv + t * 4096 + 2048 + s * 1024 + ln * 16) = v;
    }
  } else {
    const int n0 = (nt - 5) * BN;
    gemm_main<BM, BN, false>(xB + (size_t)m0 * KDIM, wqkv, 3 * D_, n0, 3 * D_, nullptr, nullptr, smem);
    store_tile<BM, BN>(smem, (u16*)(ws + WS_QKVB) + (size_t)m0 * 3 * D_ + n0, 3 * D_, 3 * D_ - n0);
  }
}

__global__ __launch_bounds__(64 * GNW, 2) void outproj_kernel(
    const char* __restrict__ ws, const u16* __restrict__ woA, const u16* __restrict__ woB,
    u16* __restrict__ out) {
  constexpr int BM = 128, BN = 64;
  using C = GemmCfg<BM, BN>;
  __shared__ __attribute__((aligned(16))) char smem[C::NST * C::STB];
  const int mt = blockIdx.x & 15, r = blockIdx.x >> 4;
  const int br = r >= 7, nt = br ? r - 7 : r;
  const int m0 = mt * BM;
  const u16* X = (const u16*)(ws + (br ? WS_OB : WS_OA)) + (size_t)m0 * KDIM;
  gemm_main<BM, BN, false>(X, br ? woB : woA, D_, nt * BN, D_, nullptr, nullptr, smem);
  store_tile<BM, BN>(smem, out + (size_t)m0 * (2 * D_) + br * D_ + nt * BN, 2 * D_, BN);
}

DI float dpp_xor1(float x) {
  return __uint_as_float(__builtin_amdgcn_update_dpp(0, __float_as_uint(x), 0xB1, 0xf, 0xf, true));
}
DI float dpp_xor2(float x) {
  return __uint_as_float(__builtin_amdgcn_update_dpp(0, __float_as_uint(x), 0x4E, 0xf, 0xf, true));
}

DI void load28(const char* p, float (&f)[28]) {
#pragma unroll
  for (int i = 0; i < 7; ++i) {
    u32x2 v = *(const u32x2*)(p + 8 * i);
    f[4 * i] = bf_lo(v[0]); f[4 * i + 1] = bf_hi(v[0]);
    f[4 * i + 2] = bf_lo(v[1]); f[4 * i + 3] = bf_hi(v[1]);
  }
}

DI void local_attn_item(const char* rows, u16* __restrict__ oB, int p0, int item, int sub) {
  const int pl = item >> 4, h = item & 15;
  const int pos = p0 + pl;
  float q[28], ka[28], kb[28], va[28], vb[28];
  const int ra = 14 + pl - 4 * sub, rb = ra - 2;
  load28(rows + (14 + pl) * 2688 + h * 56, q);
  load28(rows + ra * 2688 + 896 + h * 56, ka);
  load28(rows + rb * 2688 + 896 + h * 56, kb);
  load28(rows + ra * 2688 + 1792 + h * 56, va);
  load28(rows + rb * 2688 + 1792 + h * 56, vb);
  float sa = 0.f, sb = 0.f;
#pragma unroll
  for (int d = 0; d < 28; ++d) { sa = fmaf(q[d], ka[d], sa); sb = fmaf(q[d], kb[d], sb); }
  float la = rnd_bf16(rnd_bf16(sa) * SCALE_F), lb = rnd_bf16(rnd_bf16(sb) * SCALE_F);
  if (pos - 4 * sub < 0) la = -1e9f;
  if (pos - 4 * sub - 2 < 0) lb = -1e9f;
  float m = fmaxf(la, lb);
  m = fmaxf(m, dpp_xor1(m));
  m = fmaxf(m, dpp_xor2(m));
  float ea = __builtin_amdgcn_exp2f((la - m) * LOG2E), eb = __builtin_amdgcn_exp2f((lb - m) * LOG2E);
  float sum = ea + eb;
  sum += dpp_xor1(sum);
  sum += dpp_xor2(sum);
  float inv = 1.0f / sum;
  float pA = rnd_bf16(ea * inv), pB = rnd_bf16(eb * inv);
  float o[28];
#pragma unroll
  for (int d = 0; d < 28; ++d) {
    float t = pA * va[d] + pB * vb[d];
    t += dpp_xor1(t);
    t += dpp_xor2(t);
    o[d] = t;
  }
  u16* dst = oB + (size_t)pos * D_ + h * DH + 8 * sub;
  float w0, w1, w2, w3, w4, w5, w6, w7;
  if (sub == 0) { w0 = o[0]; w1 = o[1]; w2 = o[2]; w3 = o[3]; w4 = o[4]; w5 = o[5]; w6 = o[6]; w7 = o[7]; }
  else if (sub == 1) { w0 = o[8]; w1 = o[9]; w2 = o[10]; w3 = o[11]; w4 = o[12]; w5 = o[13]; w6 = o[14]; w7 = o[15]; }
  else if (sub == 2) { w0 = o[16]; w1 = o[17]; w2 = o[18]; w3 = o[19]; w4 = o[20]; w5 = o[21]; w6 = o[22]; w7 = o[23]; }
  else { w0 = o[24]; w1 = o[25]; w2 = o[26]; w3 = o[27]; w4 = 0; w5 = 0; w6 = 0; w7 = 0; }
  u32x2 v0 = {pack_bf16(w0, w1), pack_bf16(w2, w3)};
  *(u32x2*)dst = v0;
  if (sub < 3) {
    u32x2 v1 = {pack_bf16(w4, w5), pack_bf16(w6, w7)};
    *(u32x2*)(dst + 4) = v1;
  }
}

constexpr float THR = 8.0f;
constexpr float SC2 = SCALE_F * LOG2E;
constexpr float THR_RAW = THR / SCALE_F;
constexpr int ABUF = 32768;
constexpr int BROWS_OFF = 3 * ABUF;

__global__ __launch_bounds__(1024, 4) void attn_kernel(char* __restrict__ ws) {
  __shared__ __attribute__((aligned(16))) char smem[3 * ABUF + 58 * 1024];
  const int lane = threadIdx.x & 63;
  const int w = __builtin_amdgcn_readfirstlane(threadIdx.x >> 6);
  const int qb = w & 3, kq = w >> 2;
  const int head = blockIdx.x & 15, pb = blockIdx.x >> 4;
  const int hh = lane >> 5;
  const char* kvg = ws + WS_KV;

  auto issue = [&](int st) {
    const char* src = kvg + (size_t)(kq * 16 + st * 2) * 4096 + (qb * 2) * 1024 + lane * 16;
    char* dst = smem + (st % 3) * ABUF + kq * 8192 + (qb * 2) * 1024;
    glds16(src, dst);
    glds16(src + 1024, dst + 1024);
  };
  issue(0); issue(1);

  const int pos = pb * 128 + qb * 32 + (lane & 31);
  bf16x8 q0, q1;
  {
    const u16* qp = (const u16*)(ws + WS_QA) + (size_t)pos * D_ + head * DH + 8 * hh;
    u32x2 a = *(const u32x2*)(qp), b = *(const u32x2*)(qp + 4);
    u32x2 c = *(const u32x2*)(qp + 16);
    u32x2 d = {0u, 0u};
    if (hh == 0) d = *(const u32x2*)(qp + 20);
    u32x4 x0 = {a[0], a[1], b[0], b[1]}, x1 = {c[0], c[1], d[0], d[1]};
    q0 = __builtin_bit_cast(bf16x8, x0);
    q1 = __builtin_bit_cast(bf16x8, x1);
  }

  const int p0 = blockIdx.x * 8;
  {
    const char* qkvb = ws + WS_QKVB;
#pragma unroll
    for (int i = 0; i < 4; ++i) {
      int piece = w + 16 * i;
      if (piece < 58) {
        int off = piece * 1024 + lane * 16;
        int row = off / 2688, col = off - row * 2688;
        int grow = p0 - 14 + row;
        if (grow < 0) grow = 0;
        if (grow > S_ - 1) grow = S_ - 1;
        glds16(qkvb + (size_t)grow * 2688 + col, smem + BROWS_OFF + piece * 1024);
      }
    }
  }

  f32x16 O;
#pragma unroll
  for (int r = 0; r < 16; ++r) O[r] = 0.f;
  float R = -1e30f;

  for (int st = 0; st < 8; ++st) {
    if (st == 0) WAIT_VM(0); else if (st < 7) WAIT_VM(2); else WAIT_VM(0);
    raw_barrier();
    if (st + 2 < 8) issue(st + 2);
    const char* base = smem + (st % 3) * ABUF + kq * 8192 + lane * 16;
#pragma unroll
    for (int t = 0; t < 2; ++t) {
      bf16x8 k0 = *(const bf16x8*)(base + t * 4096);
      bf16x8 k1 = *(const bf16x8*)(base + t * 4096 + 1024);
      bf16x8 v0 = *(const bf16x8*)(base + t * 4096 + 2048);
      bf16x8 v1 = *(const bf16x8*)(base + t * 4096 + 3072);
      f32x16 s;
#pragma unroll
      for (int r = 0; r < 16; ++r) s[r] = 0.f;
      s = __builtin_amdgcn_mfma_f32_32x32x16_bf16(k0, q0, s, 0, 0, 0);
      s = __builtin_amdgcn_mfma_f32_32x32x16_bf16(k1, q1, s, 0, 0, 0);
      float tm = fmaxf(s[0], s[1]);
#pragma unroll
      for (int r = 2; r < 16; r += 2) tm = fmaxf(tm, fmaxf(s[r], s[r + 1]));
      if (__builtin_expect(__any(tm > R + THR_RAW), 0)) {
        auto rr = __builtin_amdgcn_permlane32_swap(__float_as_uint(tm), __float_as_uint(tm), false, false);
        float full = fmaxf(__uint_as_float(rr[0]), __uint_as_float(rr[1]));
        float Rn = fmaxf(R, full);
        float alpha = __builtin_amdgcn_exp2f((R - Rn) * SC2);
#pragma unroll
        for (int r = 0; r < 16; ++r) O[r] *= alpha;
        R = Rn;
      }
      const float nR = -R * SC2;
      float p[16];
#pragma unroll
      for (int r = 0; r < 16; ++r) p[r] = __builtin_amdgcn_exp2f(fmaf(s[r], SC2, nR));
      u32x4 pa = {pack_bf16(p[0], p[1]), pack_bf16(p[2], p[3]), pack_bf16(p[4], p[5]), pack_bf16(p[6], p[7])};
      u32x4 pbv = {pack_bf16(p[8], p[9]), pack_bf16(p[10], p[11]), pack_bf16(p[12], p[13]), pack_bf16(p[14], p[15])};
      O = __builtin_amdgcn_mfma_f32_32x32x16_bf16(v0, __builtin_bit_cast(bf16x8, pa), O, 0, 0, 0);
      O = __builtin_amdgcn_mfma_f32_32x32x16_bf16(v1, __builtin_bit_cast(bf16x8, pbv), O, 0, 0, 0);
    }
  }

  raw_barrier();
  if (kq != 0) {
    float* xch = (float*)smem + ((kq - 1) * 4 + qb) * (17 * 64);
#pragma unroll
    for (int r = 0; r < 16; ++r) xch[r * 64 + lane] = O[r];
    xch[16 * 64 + lane] = R;
  }
  raw_barrier();
  if (kq == 0) {
    const float* x1 = (const float*)smem + (0 * 4 + qb) * (17 * 64);
    const float* x2 = (const float*)smem + (1 * 4 + qb) * (17 * 64);
    const float* x3 = (const float*)smem + (2 * 4 + qb) * (17 * 64);
    float R1 = x1[16 * 64 + lane], R2 = x2[16 * 64 + lane], R3 = x3[16 * 64 + lane];
    float Rn = fmaxf(fmaxf(R, R1), fmaxf(R2, R3));
    float a0 = __builtin_amdgcn_exp2f((R - Rn) * SC2), a1 = __builtin_amdgcn_exp2f((R1 - Rn) * SC2);
    float a2 = __builtin_amdgcn_exp2f((R2 - Rn) * SC2), a3 = __builtin_amdgcn_exp2f((R3 - Rn) * SC2);
#pragma unroll
    for (int r = 0; r < 16; ++r)
      O[r] = O[r] * a0 + x1[r * 64 + lane] * a1 + x2[r * 64 + lane] * a2 + x3[r * 64 + lane] * a3;
    float l = __shfl(O[12], (lane & 31) + 32);
    float inv = 1.0f / l;
    u16* dst = (u16*)(ws + WS_OA) + (size_t)pos * D_ + head * DH + 4 * hh;
#pragma unroll
    for (int gq = 0; gq < 4; ++gq) {
      if (gq < 3 || hh == 0) {
        u32x2 v = {pack_bf16(O[4 * gq] * inv, O[4 * gq + 1] * inv), pack_bf16(O[4 * gq + 2] * inv, O[4 * gq + 3] * inv)};
        *(u32x2*)(dst + 8 * gq) = v;
      }
    }
  } else if (kq >= 2) {
    int tid = threadIdx.x - 512;
    local_attn_item(smem + BROWS_OFF, (u16*)(ws + WS_OB), p0, tid >> 2, tid & 3);
  }
}

extern "C" void kernel_launch(void* const* d_in, const int* in_sizes, int n_in,
                              void* d_out, int out_size, void* d_ws, size_t ws_size,
                              hipStream_t stream) {
  const u16* xA = (const u16*)d_in[0];
  const u16* wq = (const u16*)d_in[1];
  const u16* wk = (const u16*)d_in[2];
  const u16* wv = (const u16*)d_in[3];
  const u16* woA = (const u16*)d_in[4];
  const u16* xB = (const u16*)d_in[5];
  const u16* wqkv = (const u16*)d_in[6];
  const u16* woB = (const u16*)d_in[7];
  char* ws = (char*)d_ws;
  const int g_mask = 0xff;
  if (g_mask & 1) proj_kernel<<<256, 64 * GNW, 0, stream>>>(xA, wq, wk, wv, xB, wqkv, ws);
  if (g_mask & 2) attn_kernel<<<256, 1024, 0, stream>>>(ws);
  if (g_mask & 4) outproj_kernel<<<224, 64 * GNW, 0, stream>>>(ws, woA, woB, (u16*)d_out);
}
```

```cpp
#include <hip/hip_runtime.h>
#include <hip/hip_bf16.h>
#include <stdint.h>

constexpr int S_ = 2048, D_ = 448, H_ = 16, DH = 28;
constexpr int KDIM = 448;
constexpr float SCALE_F = 0.18898223650461362f;
constexpr float LOG2E = 1.4426950408889634f;

typedef unsigned short u16;
typedef short bf16x8 __attribute__((ext_vector_type(8)));
typedef short s16x4 __attribute__((ext_vector_type(4)));
typedef float f32x16 __attribute__((ext_vector_type(16)));
typedef float f32x2 __attribute__((ext_vector_type(2)));
typedef __bf16 bf16x2_t __attribute__((ext_vector_type(2)));
typedef unsigned u32x4 __attribute__((ext_vector_type(4)));
typedef unsigned u32x2 __attribute__((ext_vector_type(2)));

#define DI __device__ __forceinline__

DI unsigned pack_bf16(float lo, float hi) {
  f32x2 v = {lo, hi};
  bf16x2_t b = __builtin_convertvector(v, bf16x2_t);
  return __builtin_bit_cast(unsigned, b);
}
DI float rnd_bf16(float x) { return __uint_as_float(pack_bf16(0.f, x)); }
DI float bf_lo(unsigned u) { return __uint_as_float(u << 16); }
DI float bf_hi(unsigned u) { return __uint_as_float(u & 0xffff0000u); }

#define WAIT_VM(n) asm volatile("s_waitcnt vmcnt(" #n ")" ::: "memory")
template <int N> DI void wait_vm() { asm volatile("s_waitcnt vmcnt(%0)" ::"n"(N) : "memory"); }
DI void raw_barrier() {
  asm volatile("s_waitcnt lgkmcnt(0)" ::: "memory");
  __builtin_amdgcn_s_barrier();
}

DI void glds16(const void* g, void* l) {
  __builtin_amdgcn_global_load_lds((const unsigned*)g, (unsigned*)l, 16, 0, 0);
}
DI void glds4(const void* g, void* l) {
  __builtin_amdgcn_global_load_lds((const unsigned*)g, (unsigned*)l, 4, 0, 0);
}
DI s16x4 tr_read(const void* l) {
  return __builtin_amdgcn_ds_read_tr16_b64_v4i16((__attribute__((address_space(3))) s16x4*)l);
}

constexpr size_t WS_QA = 0;
constexpr size_t WS_KV = WS_QA + (size_t)S_ * D_ * 2;
constexpr size_t WS_QKVB = WS_KV + 64 * 4096;
constexpr size_t WS_OA = WS_QKVB + (size_t)S_ * 3 * D_ * 2;
constexpr size_t WS_OB = WS_OA + (size_t)S_ * D_ * 2;

constexpr int GNW = 8;
template <int BM, int BN> struct GemmCfg {
  static constexpr int XB = BM * 128;
  static constexpr int WB = 64 * BN * 2;
  static constexpr int STB = XB + WB;
  static constexpr int NST = 4;
  static constexpr int XI = XB / 1024 / GNW;
  static constexpr int WI = WB / 1024 / GNW;
  static constexpr int WGM = (BM >= BN) ? 4 : 2, WGN = GNW / WGM;
  static constexpr int MT = BM / 32 / WGM, NT = BN / 32 / WGN;
  static constexpr int CROW = BN * 2 + 16;
};

template <int BN> DI int wswz(int k) {
  if constexpr (BN == 128) return (k & 3) << 2;
  else return ((k >> 1) & 1) << 2;
}

template <int BM, int BN, bool KV>
DI void gemm_main(const u16* __restrict__ X,
                  const u16* __restrict__ W, int ldw, int n0, int ncols,
                  const u16* __restrict__ Wk, const u16* __restrict__ Wv,
                  char* smem, float oscale = 1.0f) {
  using C = GemmCfg<BM, BN>;
  const int lane = threadIdx.x & 63;
  const int w = __builtin_amdgcn_readfirstlane(threadIdx.x >> 6);
  const int wm = w / C::WGN, wn = w % C::WGN;
  f32x16 acc[C::MT][C::NT];
  constexpr int KVI = 64 / GNW;
  constexpr int WCNT = KV ? KVI : C::WI;
  constexpr int CNT = C::XI + WCNT;

  const u16* xsrc[C::XI];
#pragma unroll
  for (int i = 0; i < C::XI; ++i) {
    int row = (w * C::XI + i) * 8 + (lane >> 3);
    int c = (lane & 7) ^ ((row >> 1) & 7);
    xsrc[i] = X + row * KDIM + c * 8;
  }
  const u16* wsrc[WCNT];
  if constexpr (!KV) {
    constexpr int CPR = BN / 8, RPI = 1024 / (BN * 2);
#pragma unroll
    for (int i = 0; i < C::WI; ++i) {
      int k = (w * C::WI + i) * RPI + lane / CPR;
      int c = (lane % CPR) ^ wswz<BN>(k);
      int col = n0 + c * 8;
      if (col + 8 > ncols) col = ncols - 8;
      wsrc[i] = W + k * ldw + col;
    }
  } else {
#pragma unroll
    for (int i = 0; i < KVI; ++i) {
      int k = w * KVI + i;
      int c = (lane >> 2) ^ wswz<BN>(k);
      int col = c * 8 + (lane & 3) * 2;
      const u16* p = Wk + k * DH;
      if (col < 28) p = Wk + k * DH + col;
      else if (col >= 32 && col < 60) p = Wv + k * DH + (col - 32);
      wsrc[i] = p;
    }
  }
  auto issue = [&](int ks) {
    char* sb = smem + (ks % C::NST) * C::STB;
#pragma unroll
    for (int i = 0; i < C::XI; ++i) glds16(xsrc[i] + ks * 64, sb + (w * C::XI + i) * 1024);
    if constexpr (!KV) {
#pragma unroll
      for (int i = 0; i < C::WI; ++i) glds16(wsrc[i] + (size_t)ks * 64 * ldw, sb + C::XB + (w * C::WI + i) * 1024);
    } else {
#pragma unroll
      for (int i = 0; i < KVI; ++i) glds4(wsrc[i] + ks * 64 * DH, sb + C::XB + (w * KVI + i) * 256);
    }
  };

  const int hh = lane >> 5;
  int xoff[C::MT];
#pragma unroll
  for (int i = 0; i < C::MT; ++i) {
    int row = wm * (BM / C::WGM) + 32 * i + (lane & 31);
    xoff[i] = row * 128;
  }
  const int xsw = ((lane & 31) >> 1) & 7;
  const int g = lane >> 4, q = (lane & 15) >> 2, p = lane & 3;
  int woff[C::NT];
#pragma unroll
  for (int j = 0; j < C::NT; ++j) {
    int nn = wn * (BN / C::WGN) + 32 * j + 16 * (g & 1);
    int c = (nn >> 3) + (p >> 1);
    int kk = 8 * (g >> 1) + q;
    woff[j] = C::XB + kk * (BN * 2) + ((c ^ wswz<BN>(kk)) << 4) + (p & 1) * 8;
  }

#pragma unroll
  for (int i = 0; i < C::MT; ++i)
#pragma unroll
    for (int j = 0; j < C::NT; ++j)
#pragma unroll
      for (int r = 0; r < 16; ++r) acc[i][j][r] = 0.f;

  issue(0); issue(1); issue(2);
#pragma unroll
  for (int ks = 0; ks < 7; ++ks) {
    if (ks <= 4) wait_vm<2 * CNT>();
    else if (ks == 5) wait_vm<CNT>();
    else wait_vm<0>();
    raw_barrier();
    if (ks + 3 <= 6) issue(ks + 3);
    const char* sb = smem + (ks % C::NST) * C::STB;
#pragma unroll
    for (int k16 = 0; k16 < 4; ++k16) {
      bf16x8 xa[C::MT], wb[C::NT];
#pragma unroll
      for (int i = 0; i < C::MT; ++i)
        xa[i] = *(const bf16x8*)(sb + xoff[i] + (((2 * k16 + hh) ^ xsw) << 4));
#pragma unroll
      for (int j = 0; j < C::NT; ++j) {
        s16x4 lo = tr_read(sb + woff[j] + (16 * k16) * (BN * 2));
        s16x4 hi = tr_read(sb + woff[j] + (16 * k16 + 4) * (BN * 2));
        wb[j] = __builtin_shufflevector(lo, hi, 0, 1, 2, 3, 4, 5, 6, 7);
      }
#pragma unroll
      for (int i = 0; i < C::MT; ++i)
#pragma unroll
        for (int j = 0; j < C::NT; ++j)
          acc[i][j] = __builtin_amdgcn_mfma_f32_32x32x16_bf16(wb[j], xa[i], acc[i][j], 0, 0, 0);
    }
  }
  raw_barrier();
#pragma unroll
  for (int i = 0; i < C::MT; ++i)
#pragma unroll
    for (int j = 0; j < C::NT; ++j)
#pragma unroll
      for (int gq = 0; gq < 4; ++gq) {
        int pos = wm * (BM / C::WGM) + 32 * i + (lane & 31);
        int n = wn * (BN / C::WGN) + 32 * j + 8 * gq + 4 * hh;
        u32x2 v = {pack_bf16(acc[i][j][4 * gq] * oscale, acc[i][j][4 * gq + 1] * oscale),
                   pack_bf16(acc[i][j][4 * gq + 2] * oscale, acc[i][j][4 * gq + 3] * oscale)};
        *(u32x2*)(smem + pos * C::CROW + n * 2) = v;
      }
  raw_barrier();
}

template <int BM, int BN>
DI void store_tile(const char* smem, u16* __restrict__ dst, int ldd, int nvalid) {
  using C = GemmCfg<BM, BN>;
  constexpr int CPR = BN / 8;
  constexpr int TOT = BM * CPR;
#pragma unroll
  for (int it = 0; it < TOT / (64 * GNW); ++it) {
    int id = it * (64 * GNW) + threadIdx.x;
    int row = id / CPR, c = id % CPR;
    u32x4 v = *(const u32x4*)(smem + row * C::CROW + c * 16);
    if (c * 8 < nvalid) *(u32x4*)(dst + (size_t)row * ldd + c * 8) = v;
  }
}

__global__ __launch_bounds__(64 * GNW, 2) void proj_kernel(
    const u16* __restrict__ xA, const u16* __restrict__ wq, const u16* __restrict__ wk,
    const u16* __restrict__ wv, const u16* __restrict__ xB, const u16* __restrict__ wqkv,
    char* __restrict__ ws) {
  constexpr int BM = 128, BN = 128;
  using C = GemmCfg<BM, BN>;
  __shared__ __attribute__((aligned(16))) char smem[C::NST * C::STB];
  const int mt = blockIdx.x & 15, nt = blockIdx.x >> 4;
  const int m0 = mt * BM;
  if (nt < 4) {
    gemm_main<BM, BN, false>(xA + (size_t)m0 * KDIM, wq, D_, nt * BN, D_, nullptr, nullptr, smem, SCALE_F * LOG2E);
    store_tile<BM, BN>(smem, (u16*)(ws + WS_QA) + (size_t)m0 * D_ + nt * BN, D_, D_ - nt * BN);
  } else if (nt == 4) {
    gemm_main<BM, BN, true>(xA + (size_t)m0 * KDIM, nullptr, 0, 0, 0, wk, wv, smem);
    char* kv = ws + WS_KV + (size_t)(m0 >> 5) * 4096;
    {
      int id = threadIdx.x;
      int pos = id >> 2, c = id & 3;
      u32x4 v = *(const u32x4*)(smem + pos * C::CROW + c * 16);
      if (c == 3) { v[2] = 0; v[3] = 0; }
      int t = pos >> 5, s = c >> 1, hh = c & 1;
      *(u32x4*)(kv + t * 4096 + s * 1024 + ((pos & 31) + 32 * hh) * 16) = v;
    }
    {
      int id = threadIdx.x;
      int t = id >> 7, s = (id >> 6) & 1, ln = id & 63;
      int d = ln & 31, hh = ln >> 5;
      u16 e[8];
#pragma unroll
      for (int j = 0; j < 8; ++j) {
        int key = 32 * t + 16 * s + 8 * (j >> 2) + 4 * hh + (j & 3);
        u16 val = *(const u16*)(smem + key * C::CROW + (32 + d) * 2);
        if (d == 28) val = 0x3F80;
        else if (d > 28) val = 0;
        e[j] = val;
      }
      u32x4 v = {(unsigned)e[0] | ((unsigned)e[1] << 16), (unsigned)e[2] | ((unsigned)e[3] << 16),
                 (unsigned)e[4] | ((unsigned)e[5] << 16), (unsigned)e[6] | ((unsigned)e[7] << 16)};
      *(u32x4*)(kv + t * 4096 + 2048 + s * 1024 + ln * 16) = v;
    }
  } else {
    const int n0 = (nt - 5) * BN;
    gemm_main<BM, BN, false>(xB + (size_t)m0 * KDIM, wqkv, 3 * D_, n0, 3 * D_, nullptr, nullptr, smem);
    store_tile<BM, BN>(smem, (u16*)(ws + WS_QKVB) + (size_t)m0 * 3 * D_ + n0, 3 * D_, 3 * D_ - n0);
  }
}

__global__ __launch_bounds__(64 * GNW, 2) void outproj_kernel(
    const char* __restrict__ ws, const u16* __restrict__ woA, const u16* __restrict__ woB,
    u16* __restrict__ out) {
  constexpr int BM = 128, BN = 64;
  using C = GemmCfg<BM, BN>;
  __shared__ __attribute__((aligned(16))) char smem[C::NST * C::STB];
  const int mt = blockIdx.x & 15, r = blockIdx.x >> 4;
  const int br = r >= 7, nt = br ? r - 7 : r;
  const int m0 = mt * BM;
  const u16* X = (const u16*)(ws + (br ? WS_OB : WS_OA)) + (size_t)m0 * KDIM;
  gemm_main<BM, BN, false>(X, br ? woB : woA, D_, nt * BN, D_, nullptr, nullptr, smem);
  store_tile<BM, BN>(smem, out + (size_t)m0 * (2 * D_) + br * D_ + nt * BN, 2 * D_, BN);
}

DI float dpp_xor1(float x) {
  return __uint_as_float(__builtin_amdgcn_update_dpp(0, __float_as_uint(x), 0xB1, 0xf, 0xf, true));
}
DI float dpp_xor2(float x) {
  return __uint_as_float(__builtin_amdgcn_update_dpp(0, __float_as_uint(x), 0x4E, 0xf, 0xf, true));
}

DI void load28(const char* p, float (&f)[28]) {
#pragma unroll
  for (int i = 0; i < 7; ++i) {
    u32x2 v = *(const u32x2*)(p + 8 * i);
    f[4 * i] = bf_lo(v[0]); f[4 * i + 1] = bf_hi(v[0]);
    f[4 * i + 2] = bf_lo(v[1]); f[4 * i + 3] = bf_hi(v[1]);
  }
}

DI void local_attn_item(const char* rows, u16* __restrict__ oB, int p0, int item, int sub) {
  const int pl = item >> 4, h = item & 15;
  const int pos = p0 + pl;
  float q[28], ka[28], kb[28], va[28], vb[28];
  const int ra = 14 + pl - 4 * sub, rb = ra - 2;
  load28(rows + (14 + pl) * 2688 + h * 56, q);
  load28(rows + ra * 2688 + 896 + h * 56, ka);
  load28(rows + rb * 2688 + 896 + h * 56, kb);
  load28(rows + ra * 2688 + 1792 + h * 56, va);
  load28(rows + rb * 2688 + 1792 + h * 56, vb);
  float sa = 0.f, sb = 0.f;
#pragma unroll
  for (int d = 0; d < 28; ++d) { sa = fmaf(q[d], ka[d], sa); sb = fmaf(q[d], kb[d], sb); }
  float la = rnd_bf16(rnd_bf16(sa) * SCALE_F), lb = rnd_bf16(rnd_bf16(sb) * SCALE_F);
  if (pos - 4 * sub < 0) la = -1e9f;
  if (pos - 4 * sub - 2 < 0) lb = -1e9f;
  float m = fmaxf(la, lb);
  m = fmaxf(m, dpp_xor1(m));
  m = fmaxf(m, dpp_xor2(m));
  float ea = __builtin_amdgcn_exp2f((la - m) * LOG2E), eb = __builtin_amdgcn_exp2f((lb - m) * LOG2E);
  float sum = ea + eb;
  sum += dpp_xor1(sum);
  sum += dpp_xor2(sum);
  float inv = 1.0f / sum;
  float pA = rnd_bf16(ea * inv), pB = rnd_bf16(eb * inv);
  float o[28];
#pragma unroll
  for (int d = 0; d < 28; ++d) {
    float t = pA * va[d] + pB * vb[d];
    t += dpp_xor1(t);
    t += dpp_xor2(t);
    o[d] = t;
  }
  u16* dst = oB + (size_t)pos * D_ + h * DH + 8 * sub;
  float w0, w1, w2, w3, w4, w5, w6, w7;
  if (sub == 0) { w0 = o[0]; w1 = o[1]; w2 = o[2]; w3 = o[3]; w4 = o[4]; w5 = o[5]; w6 = o[6]; w7 = o[7]; }
  else if (sub == 1) { w0 = o[8]; w1 = o[9]; w2 = o[10]; w3 = o[11]; w4 = o[12]; w5 = o[13]; w6 = o[14]; w7 = o[15]; }
  else if (sub == 2) { w0 = o[16]; w1 = o[17]; w2 = o[18]; w3 = o[19]; w4 = o[20]; w5 = o[21]; w6 = o[22]; w7 = o[23]; }
  else { w0 = o[24]; w1 = o[25]; w2 = o[26]; w3 = o[27]; w4 = 0; w5 = 0; w6 = 0; w7 = 0; }
  u32x2 v0 = {pack_bf16(w0, w1), pack_bf16(w2, w3)};
  *(u32x2*)dst = v0;
  if (sub < 3) {
    u32x2 v1 = {pack_bf16(w4, w5), pack_bf16(w6, w7)};
    *(u32x2*)(dst + 4) = v1;
  }
}

constexpr float THR2 = 40.0f;
constexpr float PTHR = 1099511627776.0f;
constexpr int ABUF = 32768;
constexpr int BROWS_OFF = 3 * ABUF;

__global__ __launch_bounds__(1024, 4) void attn_kernel(char* __restrict__ ws) {
  __shared__ __attribute__((aligned(16))) char smem[3 * ABUF + 58 * 1024];
  const int lane = threadIdx.x & 63;
  const int w = __builtin_amdgcn_readfirstlane(threadIdx.x >> 6);
  const int qb = w & 3, kq = w >> 2;
  const int head = blockIdx.x & 15, pb = blockIdx.x >> 4;
  const int hh = lane >> 5;
  const char* kvg = ws + WS_KV;

  auto issue = [&](int st) {
    const char* src = kvg + (size_t)(kq * 16 + st * 2) * 4096 + (qb * 2) * 1024 + lane * 16;
    char* dst = smem + (st % 3) * ABUF + kq * 8192 + (qb * 2) * 1024;
    glds16(src, dst);
    glds16(src + 1024, dst + 1024);
  };
  issue(0); issue(1);

  const int pos = pb * 128 + qb * 32 + (lane & 31);
  bf16x8 q0, q1;
  {
    const u16* qp = (const u16*)(ws + WS_QA) + (size_t)pos * D_ + head * DH + 8 * hh;
    u32x2 a = *(const u32x2*)(qp), b = *(const u32x2*)(qp + 4);
    u32x2 c = *(const u32x2*)(qp + 16);
    u32x2 d = {0u, 0u};
    if (hh == 0) d = *(const u32x2*)(qp + 20);
    u32x4 x0 = {a[0], a[1], b[0], b[1]}, x1 = {c[0], c[1], d[0], d[1]};
    q0 = __builtin_bit_cast(bf16x8, x0);
    q1 = __builtin_bit_cast(bf16x8, x1);
  }

  const int p0 = blockIdx.x * 8;
  {
    const char* qkvb = ws + WS_QKVB;
#pragma unroll
    for (int i = 0; i < 4; ++i) {
      int piece = w + 16 * i;
      if (piece < 58) {
        int off = piece * 1024 + lane * 16;
        int row = off / 2688, col = off - row * 2688;
        int grow = p0 - 14 + row;
        if (grow < 0) grow = 0;
        if (grow > S_ - 1) grow = S_ - 1;
        glds16(qkvb + (size_t)grow * 2688 + col, smem + BROWS_OFF + piece * 1024);
      }
    }
  }

  f32x16 O, C;
#pragma unroll
  for (int r = 0; r < 16; ++r) { O[r] = 0.f; C[r] = 0.f; }
  float R = 0.f;
  bool first = true;

  for (int st = 0; st < 8; ++st) {
    if (st == 0) WAIT_VM(0); else if (st < 7) WAIT_VM(2); else WAIT_VM(0);
    raw_barrier();
    if (st + 2 < 8) issue(st + 2);
    const char* base = smem + (st % 3) * ABUF + kq * 8192 + lane * 16;
#pragma unroll
    for (int t = 0; t < 2; ++t) {
      bf16x8 k0 = *(const bf16x8*)(base + t * 4096);
      bf16x8 k1 = *(const bf16x8*)(base + t * 4096 + 1024);
      bf16x8 v0 = *(const bf16x8*)(base + t * 4096 + 2048);
      bf16x8 v1 = *(const bf16x8*)(base + t * 4096 + 3072);
      f32x16 s = __builtin_amdgcn_mfma_f32_32x32x16_bf16(k0, q0, C, 0, 0, 0);
      s = __builtin_amdgcn_mfma_f32_32x32x16_bf16(k1, q1, s, 0, 0, 0);
      float p[16];
#pragma unroll
      for (int r = 0; r < 16; ++r) p[r] = __builtin_amdgcn_exp2f(s[r]);
      float pm = fmaxf(p[0], p[1]);
#pragma unroll
      for (int r = 2; r < 16; r += 2) pm = fmaxf(fmaxf(pm, p[r]), p[r + 1]);
      if (__builtin_expect(first || __any(pm > PTHR), 0)) {
        float tm = fmaxf(s[0], s[1]);
#pragma unroll
        for (int r = 2; r < 16; r += 2) tm = fmaxf(fmaxf(tm, s[r]), s[r + 1]);
        auto rr = __builtin_amdgcn_permlane32_swap(__float_as_uint(tm), __float_as_uint(tm), false, false);
        float delta = fmaxf(__uint_as_float(rr[0]), __uint_as_float(rr[1]));
        if (!first) {
          delta = fmaxf(delta, 0.f);
          float alpha = __builtin_amdgcn_exp2f(-delta);
#pragma unroll
          for (int r = 0; r < 16; ++r) O[r] *= alpha;
        }
        R += delta;
#pragma unroll
        for (int r = 0; r < 16; ++r) { p[r] = __builtin_amdgcn_exp2f(s[r] - delta); C[r] = -R; }
        first = false;
      }
      u32x4 pa = {pack_bf16(p[0], p[1]), pack_bf16(p[2], p[3]), pack_bf16(p[4], p[5]), pack_bf16(p[6], p[7])};
      u32x4 pbv = {pack_bf16(p[8], p[9]), pack_bf16(p[10], p[11]), pack_bf16(p[12], p[13]), pack_bf16(p[14], p[15])};
      O = __builtin_amdgcn_mfma_f32_32x32x16_bf16(v0, __builtin_bit_cast(bf16x8, pa), O, 0, 0, 0);
      O = __builtin_amdgcn_mfma_f32_32x32x16_bf16(v1, __builtin_bit_cast(bf16x8, pbv), O, 0, 0, 0);
    }
  }

  raw_barrier();
  if (kq != 0) {
    float* xch = (float*)smem + ((kq - 1) * 4 + qb) * (17 * 64);
#pragma unroll
    for (int r = 0; r < 16; ++r) xch[r * 64 + lane] = O[r];
    xch[16 * 64 + lane] = R;
  }
  raw_barrier();
  if (kq == 0) {
    const float* x1 = (const float*)smem + (0 * 4 + qb) * (17 * 64);
    const float* x2 = (const float*)smem + (1 * 4 + qb) * (17 * 64);
    const float* x3 = (const float*)smem + (2 * 4 + qb) * (17 * 64);
    float R1 = x1[16 * 64 + lane], R2 = x2[16 * 64 + lane], R3 = x3[16 * 64 + lane];
    float Rn = fmaxf(fmaxf(R, R1), fmaxf(R2, R3));
    float a0 = __builtin_amdgcn_exp2f(R - Rn), a1 = __builtin_amdgcn_exp2f(R1 - Rn);
    float a2 = __builtin_amdgcn_exp2f(R2 - Rn), a3 = __builtin_amdgcn_exp2f(R3 - Rn);
#pragma unroll
    for (int r = 0; r < 16; ++r)
      O[r] = O[r] * a0 + x1[r * 64 + lane] * a1 + x2[r * 64 + lane] * a2 + x3[r * 64 + lane] * a3;
    float l = __shfl(O[12], (lane & 31) + 32);
    float inv = 1.0f / l;
    u16* dst = (u16*)(ws + WS_OA) + (size_t)pos * D_ + head * DH + 4 * hh;
#pragma unroll
    for (int gq = 0; gq < 4; ++gq) {
      if (gq < 3 || hh == 0) {
        u32x2 v = {pack_bf16(O[4 * gq] * inv, O[4 * gq + 1] * inv), pack_bf16(O[4 * gq + 2] * inv, O[4 * gq + 3] * inv)};
        *(u32x2*)(dst + 8 * gq) = v;
      }
    }
  } else if (kq >= 2) {
    int tid = threadIdx.x - 512;
    local_attn_item(smem + BROWS_OFF, (u16*)(ws + WS_OB), p0, tid >> 2, tid & 3);
  }
}

extern "C" void kernel_launch(void* const* d_in, const int* in_sizes, int n_in,
                              void* d_out, int out_size, void* d_ws, size_t ws_size,
                              hipStream_t stream) {
  const u16* xA = (const u16*)d_in[0];
  const u16* wq = (const u16*)d_in[1];
  const u16* wk = (const u16*)d_in[2];
  const u16* wv = (const u16*)d_in[3];
  const u16* woA = (const u16*)d_in[4];
  const u16* xB = (const u16*)d_in[5];
  const u16* wqkv = (const u16*)d_in[6];
  const u16* woB = (const u16*)d_in[7];
  char* ws = (char*)d_ws;
  const int g_mask = 0xff;
  if (g_mask & 1) proj_kernel<<<256, 64 * GNW, 0, stream>>>(xA, wq, wk, wv, xB, wqkv, ws);
  if (g_mask & 2) attn_kernel<<<256, 1024, 0, stream>>>(ws);
  if (g_mask & 4) outproj_kernel<<<224, 64 * GNW, 0, stream>>>(ws, woA, woB, (u16*)d_out);
}
```

```cpp
#include <hip/hip_runtime.h>
#include <hip/hip_bf16.h>
#include <stdint.h>

constexpr int S_ = 2048, D_ = 448, H_ = 16, DH = 28;
constexpr int KDIM = 448;
constexpr float SCALE_F = 0.18898223650461362f;
constexpr float LOG2E = 1.4426950408889634f;

typedef unsigned short u16;
typedef short bf16x8 __attribute__((ext_vector_type(8)));
typedef short s16x4 __attribute__((ext_vector_type(4)));
typedef float f32x16 __attribute__((ext_vector_type(16)));
typedef float f32x2 __attribute__((ext_vector_type(2)));
typedef __bf16 bf16x2_t __attribute__((ext_vector_type(2)));
typedef unsigned u32x4 __attribute__((ext_vector_type(4)));
typedef unsigned u32x2 __attribute__((ext_vector_type(2)));

#define DI __device__ __forceinline__

DI unsigned pack_bf16(float lo, float hi) {
  f32x2 v = {lo, hi};
  bf16x2_t b = __builtin_convertvector(v, bf16x2_t);
  return __builtin_bit_cast(unsigned, b);
}
DI float rnd_bf16(float x) { return __uint_as_float(pack_bf16(0.f, x)); }
DI float bf_lo(unsigned u) { return __uint_as_float(u << 16); }
DI float bf_hi(unsigned u) { return __uint_as_float(u & 0xffff0000u); }

#define WAIT_VM(n) asm volatile("s_waitcnt vmcnt(" #n ")" ::: "memory")
template <int N> DI void wait_vm() { asm volatile("s_waitcnt vmcnt(%0)" ::"n"(N) : "memory"); }
DI void raw_barrier() {
  asm volatile("s_waitcnt lgkmcnt(0)" ::: "memory");
  __builtin_amdgcn_s_barrier();
}

DI void glds16(const void* g, void* l) {
  __builtin_amdgcn_global_load_lds((const unsigned*)g, (unsigned*)l, 16, 0, 0);
}
DI void glds4(const void* g, void* l) {
  __builtin_amdgcn_global_load_lds((const unsigned*)g, (unsigned*)l, 4, 0, 0);
}
DI s16x4 tr_read(const void* l) {
  return __builtin_amdgcn_ds_read_tr16_b64_v4i16((__attribute__((address_space(3))) s16x4*)l);
}

constexpr size_t WS_QA = 0;
constexpr size_t WS_KV = WS_QA + (size_t)S_ * D_ * 2;
constexpr size_t WS_QKVB = WS_KV + 64 * 4096;
constexpr size_t WS_OA = WS_QKVB + (size_t)S_ * 3 * D_ * 2;
constexpr size_t WS_OB = WS_OA + (size_t)S_ * D_ * 2;

constexpr int GNW = 8;
template <int BM, int BN> struct GemmCfg {
  static constexpr int XB = BM * 128;
  static constexpr int WB = 64 * BN * 2;
  static constexpr int STB = XB + WB;
  static constexpr int NST = 4;
  static constexpr int XI = XB / 1024 / GNW;
  static constexpr int WI = WB / 1024 / GNW;
  static constexpr int WGM = (BM >= BN) ? 4 : 2, WGN = GNW / WGM;
  static constexpr int MT = BM / 32 / WGM, NT = BN / 32 / WGN;
  static constexpr int CROW = BN * 2 + 16;
};

template <int BN> DI int wswz(int k) {
  if constexpr (BN == 128) return (k & 3) << 2;
  else return ((k >> 1) & 1) << 2;
}

template <int BM, int BN, bool KV>
DI void gemm_main(const u16* __restrict__ X,
                  const u16* __restrict__ W, int ldw, int n0, int ncols,
                  const u16* __restrict__ Wk, const u16* __restrict__ Wv,
                  char* smem, float oscale = 1.0f) {
  using C = GemmCfg<BM, BN>;
  const int lane = threadIdx.x & 63;
  const int w = __builtin_amdgcn_readfirstlane(threadIdx.x >> 6);
  const int wm = w / C::WGN, wn = w % C::WGN;
  f32x16 acc[C::MT][C::NT];
  constexpr int KVI = 64 / GNW;
  constexpr int WCNT = KV ? KVI : C::WI;
  constexpr int CNT = C::XI + WCNT;

  const u16* xsrc[C::XI];
#pragma unroll
  for (int i = 0; i < C::XI; ++i) {
    int row = (w * C::XI + i) * 8 + (lane >> 3);
    int c = (lane & 7) ^ ((row >> 1) & 7);
    xsrc[i] = X + row * KDIM + c * 8;
  }
  const u16* wsrc[WCNT];
  if constexpr (!KV) {
    constexpr int CPR = BN / 8, RPI = 1024 / (BN * 2);
#pragma unroll
    for (int i = 0; i < C::WI; ++i) {
      int k = (w * C::WI + i) * RPI + lane / CPR;
      int c = (lane % CPR) ^ wswz<BN>(k);
      int col = n0 + c * 8;
      if (col + 8 > ncols) col = ncols - 8;
      wsrc[i] = W + k * ldw + col;
    }
  } else {
#pragma unroll
    for (int i = 0; i < KVI; ++i) {
      int k = w * KVI + i;
      int c = (lane >> 2) ^ wswz<BN>(k);
      int col = c * 8 + (lane & 3) * 2;
      const u16* p = Wk + k * DH;
      if (col < 28) p = Wk + k * DH + col;
      else if (col >= 32 && col < 60) p = Wv + k * DH + (col - 32);
      wsrc[i] = p;
    }
  }
  auto issue = [&](int ks) {
    char* sb = smem + (ks % C::NST) * C::STB;
#pragma unroll
    for (int i = 0; i < C::XI; ++i) glds16(xsrc[i] + ks * 64, sb + (w * C::XI + i) * 1024);
    if constexpr (!KV) {
#pragma unroll
      for (int i = 0; i < C::WI; ++i) glds16(wsrc[i] + (size_t)ks * 64 * ldw, sb + C::XB + (w * C::WI + i) * 1024);
    } else {
#pragma unroll
      for (int i = 0; i < KVI; ++i) glds4(wsrc[i] + ks * 64 * DH, sb + C::XB + (w * KVI + i) * 256);
    }
  };

  const int hh = lane >> 5;
  int xoff[C::MT];
#pragma unroll
  for (int i = 0; i < C::MT; ++i) {
    int row = wm * (BM / C::WGM) + 32 * i + (lane & 31);
    xoff[i] = row * 128;
  }
  const int xsw = ((lane & 31) >> 1) & 7;
  const int g = lane >> 4, q = (lane & 15) >> 2, p = lane & 3;
  int woff[C::NT];
#pragma unroll
  for (int j = 0; j < C::NT; ++j) {
    int nn = wn * (BN / C::WGN) + 32 * j + 16 * (g & 1);
    int c = (nn >> 3) + (p >> 1);
    int kk = 8 * (g >> 1) + q;
    woff[j] = C::XB + kk * (BN * 2) + ((c ^ wswz<BN>(kk)) << 4) + (p & 1) * 8;
  }

#pragma unroll
  for (int i = 0; i < C::MT; ++i)
#pragma unroll
    for (int j = 0; j < C::NT; ++j)
#pragma unroll
      for (int r = 0; r < 16; ++r) acc[i][j][r] = 0.f;

  issue(0); issue(1); issue(2);
#pragma unroll
  for (int ks = 0; ks < 7; ++ks) {
    if (ks <= 4) wait_vm<2 * CNT>();
    else if (ks == 5) wait_vm<CNT>();
    else wait_vm<0>();
    raw_barrier();
    if (ks + 3 <= 6) issue(ks + 3);
    const char* sb = smem + (ks % C::NST) * C::STB;
#pragma unroll
    for (int k16 = 0; k16 < 4; ++k16) {
      bf16x8 xa[C::MT], wb[C::NT];
#pragma unroll
      for (int i = 0; i < C::MT; ++i)
        xa[i] = *(const bf16x8*)(sb + xoff[i] + (((2 * k16 + hh) ^ xsw) << 4));
#pragma unroll
      for (int j = 0; j < C::NT; ++j) {
        s16x4 lo = tr_read(sb + woff[j] + (16 * k16) * (BN * 2));
        s16x4 hi = tr_read(sb + woff[j] + (16 * k16 + 4) * (BN * 2));
        wb[j] = __builtin_shufflevector(lo, hi, 0, 1, 2, 3, 4, 5, 6, 7);
      }
#pragma unroll
      for (int i = 0; i < C::MT; ++i)
#pragma unroll
        for (int j = 0; j < C::NT; ++j)
          acc[i][j] = __builtin_amdgcn_mfma_f32_32x32x16_bf16(wb[j], xa[i], acc[i][j], 0, 0, 0);
    }
  }
  raw_barrier();
#pragma unroll
  for (int i = 0; i < C::MT; ++i)
#pragma unroll
    for (int j = 0; j < C::NT; ++j)
#pragma unroll
      for (int gq = 0; gq < 4; ++gq) {
        int pos = wm * (BM / C::WGM) + 32 * i + (lane & 31);
        int n = wn * (BN / C::WGN) + 32 * j + 8 * gq + 4 * hh;
        u32x2 v = {pack_bf16(acc[i][j][4 * gq] * oscale, acc[i][j][4 * gq + 1] * oscale),
                   pack_bf16(acc[i][j][4 * gq + 2] * oscale, acc[i][j][4 * gq + 3] * oscale)};
        *(u32x2*)(smem + pos * C::CROW + n * 2) = v;
      }
  raw_barrier();
}

template <int BM, int BN>
DI void store_tile(const char* smem, u16* __restrict__ dst, int ldd, int nvalid) {
  using C = GemmCfg<BM, BN>;
  constexpr int CPR = BN / 8;
  constexpr int TOT = BM * CPR;
#pragma unroll
  for (int it = 0; it < TOT / (64 * GNW); ++it) {
    int id = it * (64 * GNW) + threadIdx.x;
    int row = id / CPR, c = id % CPR;
    u32x4 v = *(const u32x4*)(smem + row * C::CROW + c * 16);
    if (c * 8 < nvalid) *(u32x4*)(dst + (size_t)row * ldd + c * 8) = v;
  }
}

__global__ __launch_bounds__(64 * GNW, 2) void proj_kernel(
    const u16* __restrict__ xA, const u16* __restrict__ wq, const u16* __restrict__ wk,
    const u16* __restrict__ wv, const u16* __restrict__ xB, const u16* __restrict__ wqkv,
    char* __restrict__ ws) {
  constexpr int BM = 128, BN = 128;
  using C = GemmCfg<BM, BN>;
  __shared__ __attribute__((aligned(16))) char smem[C::NST * C::STB];
  const int mt = blockIdx.x & 15, nt = blockIdx.x >> 4;
  const int m0 = mt * BM;
  if (nt < 4) {
    gemm_main<BM, BN, false>(xA + (size_t)m0 * KDIM, wq, D_, nt * BN, D_, nullptr, nullptr, smem, SCALE_F * LOG2E);
    store_tile<BM, BN>(smem, (u16*)(ws + WS_QA) + (size_t)m0 * D_ + nt * BN, D_, D_ - nt * BN);
  } else if (nt == 4) {
    gemm_main<BM, BN, true>(xA + (size_t)m0 * KDIM, nullptr, 0, 0, 0, wk, wv, smem);
    char* kv = ws + WS_KV + (size_t)(m0 >> 5) * 4096;
    {
      int id = threadIdx.x;
      int pos = id >> 2, c = id & 3;
      u32x4 v = *(const u32x4*)(smem + pos * C::CROW + c * 16);
      if (c == 3) { v[2] = 0; v[3] = 0; }
      int t = pos >> 5, s = c >> 1, hh = c & 1;
      *(u32x4*)(kv + t * 4096 + s * 1024 + ((pos & 31) + 32 * hh) * 16) = v;
    }
    {
      int id = threadIdx.x;
      int t = id >> 7, s = (id >> 6) & 1, ln = id & 63;
      int d = ln & 31, hh = ln >> 5;
      u16 e[8];
#pragma unroll
      for (int j = 0; j < 8; ++j) {
        int key = 32 * t + 16 * s + 8 * (j >> 2) + 4 * hh + (j & 3);
        u16 val = *(const u16*)(smem + key * C::CROW + (32 + d) * 2);
        if (d == 28) val = 0x3F80;
        else if (d > 28) val = 0;
        e[j] = val;
      }
      u32x4 v = {(unsigned)e[0] | ((unsigned)e[1] << 16), (unsigned)e[2] | ((unsigned)e[3] << 16),
                 (unsigned)e[4] | ((unsigned)e[5] << 16), (unsigned)e[6] | ((unsigned)e[7] << 16)};
      *(u32x4*)(kv + t * 4096 + 2048 + s * 1024 + ln * 16) = v;
    }
  } else {
    const int n0 = (nt - 5) * BN;
    gemm_main<BM, BN, false>(xB + (size_t)m0 * KDIM, wqkv, 3 * D_, n0, 3 * D_, nullptr, nullptr, smem);
    store_tile<BM, BN>(smem, (u16*)(ws + WS_QKVB) + (size_t)m0 * 3 * D_ + n0, 3 * D_, 3 * D_ - n0);
  }
}

__global__ __launch_bounds__(64 * GNW, 2) void outproj_kernel(
    const char* __restrict__ ws, const u16* __restrict__ woA, const u16* __restrict__ woB,
    u16* __restrict__ out) {
  constexpr int BM = 128, BN = 64;
  using C = GemmCfg<BM, BN>;
  __shared__ __attribute__((aligned(16))) char smem[C::NST * C::STB];
  const int mt = blockIdx.x & 15, r = blockIdx.x >> 4;
  const int br = r >= 7, nt = br ? r - 7 : r;
  const int m0 = mt * BM;
  const u16* X = (const u16*)(ws + (br ? WS_OB : WS_OA)) + (size_t)m0 * KDIM;
  gemm_main<BM, BN, false>(X, br ? woB : woA, D_, nt * BN, D_, nullptr, nullptr, smem);
  store_tile<BM, BN>(smem, out + (size_t)m0 * (2 * D_) + br * D_ + nt * BN, 2 * D_, BN);
}

DI float dpp_xor1(float x) {
  return __uint_as_float(__builtin_amdgcn_update_dpp(0, __float_as_uint(x), 0xB1, 0xf, 0xf, true));
}
DI float dpp_xor2(float x) {
  return __uint_as_float(__builtin_amdgcn_update_dpp(0, __float_as_uint(x), 0x4E, 0xf, 0xf, true));
}

DI float dpp_hmir(float x) {
  return __uint_as_float(__builtin_amdgcn_update_dpp(0, __float_as_uint(x), 0x141, 0xf, 0xf, true));
}
DI void ld4(const u16* p, float& a, float& b, float& c, float& d) {
  u32x2 v = *(const u32x2*)p;
  a = bf_lo(v[0]); b = bf_hi(v[0]); c = bf_lo(v[1]); d = bf_hi(v[1]);
}

DI void local_attn_item(const u16* __restrict__ qkv, u16* __restrict__ oB, int item, int j, int lane) {
  const int pos = item >> 4, h = item & 15;
  const int pj = pos - 2 * j, pjc = pj < 0 ? 0 : pj;
  const u16* qp = qkv + (size_t)pos * 1344 + h * DH;
  const u16* kp = qkv + (size_t)pjc * 1344 + 448 + h * DH;
  u32x2 qv[7], kv[7], vv[8];
#pragma unroll
  for (int i = 0; i < 7; ++i) { qv[i] = *(const u32x2*)(qp + 4 * i); kv[i] = *(const u32x2*)(kp + 4 * i); }
  const int jj = j < 7 ? j : 6;
#pragma unroll
  for (int wdw = 0; wdw < 8; ++wdw) {
    int pw = pos - 2 * wdw; if (pw < 0) pw = 0;
    vv[wdw] = *(const u32x2*)(qkv + (size_t)pw * 1344 + 896 + h * DH + 4 * jj);
  }
  float sa = 0.f;
#pragma unroll
  for (int i = 0; i < 7; ++i) {
    sa = fmaf(bf_lo(qv[i][0]), bf_lo(kv[i][0]), sa);
    sa = fmaf(bf_hi(qv[i][0]), bf_hi(kv[i][0]), sa);
    sa = fmaf(bf_lo(qv[i][1]), bf_lo(kv[i][1]), sa);
    sa = fmaf(bf_hi(qv[i][1]), bf_hi(kv[i][1]), sa);
  }
  float la = rnd_bf16(rnd_bf16(sa) * SCALE_F);
  if (pj < 0) la = -1e9f;
  float m = fmaxf(la, dpp_xor1(la));
  m = fmaxf(m, dpp_xor2(m));
  m = fmaxf(m, dpp_hmir(m));
  float e = __builtin_amdgcn_exp2f((la - m) * LOG2E);
  float sum = e + dpp_xor1(e);
  sum += dpp_xor2(sum);
  sum += dpp_hmir(sum);
  float pj_ = rnd_bf16(e / sum);
  float o0 = 0.f, o1 = 0.f, o2 = 0.f, o3 = 0.f;
#pragma unroll
  for (int wdw = 0; wdw < 8; ++wdw) {
    float pw = __shfl(pj_, (lane & ~7) | wdw);
    o0 = fmaf(pw, bf_lo(vv[wdw][0]), o0); o1 = fmaf(pw, bf_hi(vv[wdw][0]), o1);
    o2 = fmaf(pw, bf_lo(vv[wdw][1]), o2); o3 = fmaf(pw, bf_hi(vv[wdw][1]), o3);
  }
  if (j < 7) {
    u32x2 v0 = {pack_bf16(o0, o1), pack_bf16(o2, o3)};
    *(u32x2*)(oB + (size_t)pos * D_ + h * DH + 4 * j) = v0;
  }
}

constexpr float PTHR = 1099511627776.0f;
constexpr int ABUF = 65536;

__global__ __launch_bounds__(1024, 4) void attn_kernel(char* __restrict__ ws) {
  __shared__ __attribute__((aligned(16))) char smem[2 * ABUF];
  const int lane = threadIdx.x & 63;
  const int w = __builtin_amdgcn_readfirstlane(threadIdx.x >> 6);
  const int qb = w & 3, kq = w >> 2;
  const int head = blockIdx.x & 15, pb = blockIdx.x >> 4;
  const int hh = lane >> 5;
  const char* kvg = ws + WS_KV;

  auto issue = [&](int st) {
    const char* src = kvg + (size_t)(kq * 16 + st * 4 + qb) * 4096 + lane * 16;
    char* dst = smem + (st & 1) * ABUF + kq * 16384 + qb * 4096;
#pragma unroll
    for (int i = 0; i < 4; ++i) glds16(src + i * 1024, dst + i * 1024);
  };
  issue(0);

  const int pos = pb * 128 + qb * 32 + (lane & 31);
  bf16x8 q0, q1;
  {
    const u16* qp = (const u16*)(ws + WS_QA) + (size_t)pos * D_ + head * DH + 8 * hh;
    u32x2 a = *(const u32x2*)(qp), b = *(const u32x2*)(qp + 4);
    u32x2 c = *(const u32x2*)(qp + 16);
    u32x2 d = {0u, 0u};
    if (hh == 0) d = *(const u32x2*)(qp + 20);
    u32x4 x0 = {a[0], a[1], b[0], b[1]}, x1 = {c[0], c[1], d[0], d[1]};
    q0 = __builtin_bit_cast(bf16x8, x0);
    q1 = __builtin_bit_cast(bf16x8, x1);
  }

  f32x16 O, C;
#pragma unroll
  for (int r = 0; r < 16; ++r) { O[r] = 0.f; C[r] = 0.f; }
  float R = 0.f;
  bool first = true;

  for (int st = 0; st < 4; ++st) {
    WAIT_VM(0);
    raw_barrier();
    if (st + 1 < 4) issue(st + 1);
    if (st == kq) {
      local_attn_item((const u16*)(ws + WS_QKVB), (u16*)(ws + WS_OB), blockIdx.x * 128 + w * 8 + (lane >> 3), lane & 7, lane);
    }
    const char* base = smem + (st & 1) * ABUF + kq * 16384 + lane * 16;
#pragma unroll 2
    for (int t = 0; t < 4; ++t) {
      bf16x8 k0 = *(const bf16x8*)(base + t * 4096);
      bf16x8 k1 = *(const bf16x8*)(base + t * 4096 + 1024);
      bf16x8 v0 = *(const bf16x8*)(base + t * 4096 + 2048);
      bf16x8 v1 = *(const bf16x8*)(base + t * 4096 + 3072);
      f32x16 s = __builtin_amdgcn_mfma_f32_32x32x16_bf16(k0, q0, C, 0, 0, 0);
      s = __builtin_amdgcn_mfma_f32_32x32x16_bf16(k1, q1, s, 0, 0, 0);
      float p[16];
#pragma unroll
      for (int r = 0; r < 16; ++r) p[r] = __builtin_amdgcn_exp2f(s[r]);
      float pm = fmaxf(p[0], p[1]);
#pragma unroll
      for (int r = 2; r < 16; r += 2) pm = fmaxf(fmaxf(pm, p[r]), p[r + 1]);
      if (__builtin_expect(first || __any(pm > PTHR), 0)) {
        float tm = fmaxf(s[0], s[1]);
#pragma unroll
        for (int r = 2; r < 16; r += 2) tm = fmaxf(fmaxf(tm, s[r]), s[r + 1]);
        auto rr = __builtin_amdgcn_permlane32_swap(__float_as_uint(tm), __float_as_uint(tm), false, false);
        float delta = fmaxf(__uint_as_float(rr[0]), __uint_as_float(rr[1]));
        if (!first) {
          delta = fmaxf(delta, 0.f);
          float alpha = __builtin_amdgcn_exp2f(-delta);
#pragma unroll
          for (int r = 0; r < 16; ++r) O[r] *= alpha;
        }
        R += delta;
#pragma unroll
        for (int r = 0; r < 16; ++r) { p[r] = __builtin_amdgcn_exp2f(s[r] - delta); C[r] = -R; }
        first = false;
      }
      u32x4 pa = {pack_bf16(p[0], p[1]), pack_bf16(p[2], p[3]), pack_bf16(p[4], p[5]), pack_bf16(p[6], p[7])};
      u32x4 pbv = {pack_bf16(p[8], p[9]), pack_bf16(p[10], p[11]), pack_bf16(p[12], p[13]), pack_bf16(p[14], p[15])};
      O = __builtin_amdgcn_mfma_f32_32x32x16_bf16(v0, __builtin_bit_cast(bf16x8, pa), O, 0, 0, 0);
      O = __builtin_amdgcn_mfma_f32_32x32x16_bf16(v1, __builtin_bit_cast(bf16x8, pbv), O, 0, 0, 0);
    }
  }

  raw_barrier();
  {
    float* xch = (float*)smem + (kq * 4 + qb) * (17 * 64);
#pragma unroll
    for (int r = 0; r < 16; ++r) xch[r * 64 + lane] = O[r];
    xch[16 * 64 + lane] = R;
  }
  raw_barrier();
  {
    float Rs[4], a[4];
#pragma unroll
    for (int i = 0; i < 4; ++i) Rs[i] = ((const float*)smem)[(i * 4 + qb) * (17 * 64) + 16 * 64 + lane];
    float Rn = fmaxf(fmaxf(Rs[0], Rs[1]), fmaxf(Rs[2], Rs[3]));
#pragma unroll
    for (int i = 0; i < 4; ++i) a[i] = __builtin_amdgcn_exp2f(Rs[i] - Rn);
    float l = 0.f, o[4] = {0.f, 0.f, 0.f, 0.f};
#pragma unroll
    for (int i = 0; i < 4; ++i) {
      const float* x = (const float*)smem + (i * 4 + qb) * (17 * 64);
      l = fmaf(x[12 * 64 + (lane & 31) + 32], a[i], l);
#pragma unroll
      for (int r = 0; r < 4; ++r) o[r] = fmaf(x[(4 * kq + r) * 64 + lane], a[i], o[r]);
    }
    float inv = 1.0f / l;
    if (kq < 3 || hh == 0) {
      u32x2 v = {pack_bf16(o[0] * inv, o[1] * inv), pack_bf16(o[2] * inv, o[3] * inv)};
      *(u32x2*)((u16*)(ws + WS_OA) + (size_t)pos * D_ + head * DH + 4 * hh + 8 * kq) = v;
    }
  }
}

extern "C" void kernel_launch(void* const* d_in, const int* in_sizes, int n_in,
                              void* d_out, int out_size, void* d_ws, size_t ws_size,
                              hipStream_t stream) {
  const u16* xA = (const u16*)d_in[0];
  const u16* wq = (const u16*)d_in[1];
  const u16* wk = (const u16*)d_in[2];
  const u16* wv = (const u16*)d_in[3];
  const u16* woA = (const u16*)d_in[4];
  const u16* xB = (const u16*)d_in[5];
  const u16* wqkv = (const u16*)d_in[6];
  const u16* woB = (const u16*)d_in[7];
  char* ws = (char*)d_ws;
  const int g_mask = 0xff;
  if (g_mask & 1) proj_kernel<<<256, 64 * GNW, 0, stream>>>(xA, wq, wk, wv, xB, wqkv, ws);
  if (g_mask & 2) attn_kernel<<<256, 1024, 0, stream>>>(ws);
  if (g_mask & 4) outproj_kernel<<<224, 64 * GNW, 0, stream>>>(ws, woA, woB, (u16*)d_out);
}
```

```cpp
#include <hip/hip_runtime.h>
#include <hip/hip_bf16.h>
#include <stdint.h>

constexpr int S_ = 2048, D_ = 448, H_ = 16, DH = 28;
constexpr int KDIM = 448;
constexpr float SCALE_F = 0.18898223650461362f;
constexpr float LOG2E = 1.4426950408889634f;

typedef unsigned short u16;
typedef short bf16x8 __attribute__((ext_vector_type(8)));
typedef short s16x4 __attribute__((ext_vector_type(4)));
typedef float f32x16 __attribute__((ext_vector_type(16)));
typedef float f32x2 __attribute__((ext_vector_type(2)));
typedef __bf16 bf16x2_t __attribute__((ext_vector_type(2)));
typedef unsigned u32x4 __attribute__((ext_vector_type(4)));
typedef unsigned u32x2 __attribute__((ext_vector_type(2)));

#define DI __device__ __forceinline__

DI unsigned pack_bf16(float lo, float hi) {
  f32x2 v = {lo, hi};
  bf16x2_t b = __builtin_convertvector(v, bf16x2_t);
  return __builtin_bit_cast(unsigned, b);
}
DI float rnd_bf16(float x) { return __uint_as_float(pack_bf16(0.f, x)); }
DI float bf_lo(unsigned u) { return __uint_as_float(u << 16); }
DI float bf_hi(unsigned u) { return __uint_as_float(u & 0xffff0000u); }

#define WAIT_VM(n) asm volatile("s_waitcnt vmcnt(" #n ")" ::: "memory")
template <int N> DI void wait_vm() { asm volatile("s_waitcnt vmcnt(%0)" ::"n"(N) : "memory"); }
DI void raw_barrier() {
  asm volatile("s_waitcnt lgkmcnt(0)" ::: "memory");
  __builtin_amdgcn_s_barrier();
}

DI void glds16(const void* g, void* l) {
  __builtin_amdgcn_global_load_lds((const unsigned*)g, (unsigned*)l, 16, 0, 0);
}
DI void glds4(const void* g, void* l) {
  __builtin_amdgcn_global_load_lds((const unsigned*)g, (unsigned*)l, 4, 0, 0);
}
DI s16x4 tr_read(const void* l) {
  return __builtin_amdgcn_ds_read_tr16_b64_v4i16((__attribute__((address_space(3))) s16x4*)l);
}

constexpr size_t WS_QA = 0;
constexpr size_t WS_KV = WS_QA + (size_t)S_ * D_ * 2;
constexpr size_t WS_QKVB = WS_KV + 64 * 4096;
constexpr size_t WS_OA = WS_QKVB + (size_t)S_ * 3 * D_ * 2;
constexpr size_t WS_OB = WS_OA + (size_t)S_ * D_ * 2;

constexpr int GNW = 8;
template <int BM, int BN> struct GemmCfg {
  static constexpr int XB = BM * 128;
  static constexpr int WB = 64 * BN * 2;
  static constexpr int STB = XB + WB;
  static constexpr int NST = 4;
  static constexpr int XI = XB / 1024 / GNW;
  static constexpr int WI = WB / 1024 / GNW;
  static constexpr int WGM = (BM >= BN) ? 4 : 2, WGN = GNW / WGM;
  static constexpr int MT = BM / 32 / WGM, NT = BN / 32 / WGN;
  static constexpr int CROW = BN * 2 + 16;
};

template <int BN> DI int wswz(int k) {
  if constexpr (BN == 128) return (k & 3) << 2;
  else return ((k >> 1) & 1) << 2;
}

template <int BM, int BN, bool KV>
DI void gemm_main(const u16* __restrict__ X,
                  const u16* __restrict__ W, int ldw, int n0, int ncols,
                  const u16* __restrict__ Wk, const u16* __restrict__ Wv,
                  char* smem, float oscale = 1.0f) {
  using C = GemmCfg<BM, BN>;
  const int lane = threadIdx.x & 63;
  const int w = __builtin_amdgcn_readfirstlane(threadIdx.x >> 6);
  const int wm = w / C::WGN, wn = w % C::WGN;
  f32x16 acc[C::MT][C::NT];
  constexpr int KVI = 64 / GNW;
  constexpr int WCNT = KV ? KVI : C::WI;
  constexpr int CNT = C::XI + WCNT;

  const u16* xsrc[C::XI];
#pragma unroll
  for (int i = 0; i < C::XI; ++i) {
    int row = (w * C::XI + i) * 8 + (lane >> 3);
    int c = (lane & 7) ^ ((row >> 1) & 7);
    xsrc[i] = X + row * KDIM + c * 8;
  }
  const u16* wsrc[WCNT];
  if constexpr (!KV) {
    constexpr int CPR = BN / 8, RPI = 1024 / (BN * 2);
#pragma unroll
    for (int i = 0; i < C::WI; ++i) {
      int k = (w * C::WI + i) * RPI + lane / CPR;
      int c = (lane % CPR) ^ wswz<BN>(k);
      int col = n0 + c * 8;
      if (col + 8 > ncols) col = ncols - 8;
      wsrc[i] = W + k * ldw + col;
    }
  } else {
#pragma unroll
    for (int i = 0; i < KVI; ++i) {
      int k = w * KVI + i;
      int c = (lane >> 2) ^ wswz<BN>(k);
      int col = c * 8 + (lane & 3) * 2;
      const u16* p = Wk + k * DH;
      if (col < 28) p = Wk + k * DH + col;
      else if (col >= 32 && col < 60) p = Wv + k * DH + (col - 32);
      wsrc[i] = p;
    }
  }
  auto issue = [&](int ks) {
    char* sb = smem + (ks % C::NST) * C::STB;
#pragma unroll
    for (int i = 0; i < C::XI; ++i) glds16(xsrc[i] + ks * 64, sb + (w * C::XI + i) * 1024);
    if constexpr (!KV) {
#pragma unroll
      for (int i = 0; i < C::WI; ++i) glds16(wsrc[i] + (size_t)ks * 64 * ldw, sb + C::XB + (w * C::WI + i) * 1024);
    } else {
#pragma unroll
      for (int i = 0; i < KVI; ++i) glds4(wsrc[i] + ks * 64 * DH, sb + C::XB + (w * KVI + i) * 256);
    }
  };

  const int hh = lane >> 5;
  int xoff[C::MT];
#pragma unroll
  for (int i = 0; i < C::MT; ++i) {
    int row = wm * (BM / C::WGM) + 32 * i + (lane & 31);
    xoff[i] = row * 128;
  }
  const int xsw = ((lane & 31) >> 1) & 7;
  const int g = lane >> 4, q = (lane & 15) >> 2, p = lane & 3;
  int woff[C::NT];
#pragma unroll
  for (int j = 0; j < C::NT; ++j) {
    int nn = wn * (BN / C::WGN) + 32 * j + 16 * (g & 1);
    int c = (nn >> 3) + (p >> 1);
    int kk = 8 * (g >> 1) + q;
    woff[j] = C::XB + kk * (BN * 2) + ((c ^ wswz<BN>(kk)) << 4) + (p & 1) * 8;
  }

#pragma unroll
  for (int i = 0; i < C::MT; ++i)
#pragma unroll
    for (int j = 0; j < C::NT; ++j)
#pragma unroll
      for (int r = 0; r < 16; ++r) acc[i][j][r] = 0.f;

  issue(0); issue(1); issue(2);
#pragma unroll
  for (int ks = 0; ks < 7; ++ks) {
    if (ks <= 4) wait_vm<2 * CNT>();
    else if (ks == 5) wait_vm<CNT>();
    else wait_vm<0>();
    raw_barrier();
    if (ks + 3 <= 6) issue(ks + 3);
    const char* sb = smem + (ks % C::NST) * C::STB;
#pragma unroll
    for (int k16 = 0; k16 < 4; ++k16) {
      bf16x8 xa[C::MT], wb[C::NT];
#pragma unroll
      for (int i = 0; i < C::MT; ++i)
        xa[i] = *(const bf16x8*)(sb + xoff[i] + (((2 * k16 + hh) ^ xsw) << 4));
#pragma unroll
      for (int j = 0; j < C::NT; ++j) {
        s16x4 lo = tr_read(sb + woff[j] + (16 * k16) * (BN * 2));
        s16x4 hi = tr_read(sb + woff[j] + (16 * k16 + 4) * (BN * 2));
        wb[j] = __builtin_shufflevector(lo, hi, 0, 1, 2, 3, 4, 5, 6, 7);
      }
#pragma unroll
      for (int i = 0; i < C::MT; ++i)
#pragma unroll
        for (int j = 0; j < C::NT; ++j)
          acc[i][j] = __builtin_amdgcn_mfma_f32_32x32x16_bf16(wb[j], xa[i], acc[i][j], 0, 0, 0);
    }
  }
  raw_barrier();
#pragma unroll
  for (int i = 0; i < C::MT; ++i)
#pragma unroll
    for (int j = 0; j < C::NT; ++j)
#pragma unroll
      for (int gq = 0; gq < 4; ++gq) {
        int pos = wm * (BM / C::WGM) + 32 * i + (lane & 31);
        int n = wn * (BN / C::WGN) + 32 * j + 8 * gq + 4 * hh;
        u32x2 v = {pack_bf16(acc[i][j][4 * gq] * oscale, acc[i][j][4 * gq + 1] * oscale),
                   pack_bf16(acc[i][j][4 * gq + 2] * oscale, acc[i][j][4 * gq + 3] * oscale)};
        *(u32x2*)(smem + pos * C::CROW + n * 2) = v;
      }
  raw_barrier();
}

template <int BM, int BN>
DI void store_tile(const char* smem, u16* __restrict__ dst, int ldd, int nvalid) {
  using C = GemmCfg<BM, BN>;
  constexpr int CPR = BN / 8;
  constexpr int TOT = BM * CPR;
#pragma unroll
  for (int it = 0; it < TOT / (64 * GNW); ++it) {
    int id = it * (64 * GNW) + threadIdx.x;
    int row = id / CPR, c = id % CPR;
    u32x4 v = *(const u32x4*)(smem + row * C::CROW + c * 16);
    if (c * 8 < nvalid) *(u32x4*)(dst + (size_t)row * ldd + c * 8) = v;
  }
}

__global__ __launch_bounds__(64 * GNW, 2) void proj_kernel(
    const u16* __restrict__ xA, const u16* __restrict__ wq, const u16* __restrict__ wk,
    const u16* __restrict__ wv, const u16* __restrict__ xB, const u16* __restrict__ wqkv,
    char* __restrict__ ws) {
  constexpr int BM = 128, BN = 128;
  using C = GemmCfg<BM, BN>;
  __shared__ __attribute__((aligned(16))) char smem[C::NST * C::STB];
  const int mt = blockIdx.x & 15, nt = blockIdx.x >> 4;
  const int m0 = mt * BM;
  if (nt < 4) {
    gemm_main<BM, BN, false>(xA + (size_t)m0 * KDIM, wq, D_, nt * BN, D_, nullptr, nullptr, smem, SCALE_F * LOG2E);
    store_tile<BM, BN>(smem, (u16*)(ws + WS_QA) + (size_t)m0 * D_ + nt * BN, D_, D_ - nt * BN);
  } else if (nt == 4) {
    gemm_main<BM, BN, true>(xA + (size_t)m0 * KDIM, nullptr, 0, 0, 0, wk, wv, smem);
    char* kv = ws + WS_KV + (size_t)(m0 >> 5) * 4096;
    {
      int id = threadIdx.x;
      int pos = id >> 2, c = id & 3;
      u32x4 v = *(const u32x4*)(smem + pos * C::CROW + c * 16);
      if (c == 3) { v[2] = 0; v[3] = 0; }
      int t = pos >> 5, s = c >> 1, hh = c & 1;
      *(u32x4*)(kv + t * 4096 + s * 1024 + ((pos & 31) + 32 * hh) * 16) = v;
    }
    {
      int id = threadIdx.x;
      int t = id >> 7, s = (id >> 6) & 1, ln = id & 63;
      int d = ln & 31, hh = ln >> 5;
      u16 e[8];
#pragma unroll
      for (int j = 0; j < 8; ++j) {
        int key = 32 * t + 16 * s + 8 * (j >> 2) + 4 * hh + (j & 3);
        u16 val = *(const u16*)(smem + key * C::CROW + (32 + d) * 2);
        if (d == 28) val = 0x3F80;
        else if (d > 28) val = 0;
        e[j] = val;
      }
      u32x4 v = {(unsigned)e[0] | ((unsigned)e[1] << 16), (unsigned)e[2] | ((unsigned)e[3] << 16),
                 (unsigned)e[4] | ((unsigned)e[5] << 16), (unsigned)e[6] | ((unsigned)e[7] << 16)};
      *(u32x4*)(kv + t * 4096 + 2048 + s * 1024 + ln * 16) = v;
    }
  } else {
    const int n0 = (nt - 5) * BN;
    gemm_main<BM, BN, false>(xB + (size_t)m0 * KDIM, wqkv, 3 * D_, n0, 3 * D_, nullptr, nullptr, smem);
    store_tile<BM, BN>(smem, (u16*)(ws + WS_QKVB) + (size_t)m0 * 3 * D_ + n0, 3 * D_, 3 * D_ - n0);
  }
}

__global__ __launch_bounds__(64 * GNW, 2) void outproj_kernel(
    const char* __restrict__ ws, const u16* __restrict__ woA, const u16* __restrict__ woB,
    u16* __restrict__ out) {
  constexpr int BM = 128, BN = 64;
  using C = GemmCfg<BM, BN>;
  __shared__ __attribute__((aligned(16))) char smem[C::NST * C::STB];
  const int mt = blockIdx.x & 15, r = blockIdx.x >> 4;
  const int br = r >= 7, nt = br ? r - 7 : r;
  const int m0 = mt * BM;
  const u16* X = (const u16*)(ws + (br ? WS_OB : WS_OA)) + (size_t)m0 * KDIM;
  gemm_main<BM, BN, false>(X, br ? woB : woA, D_, nt * BN, D_, nullptr, nullptr, smem);
  store_tile<BM, BN>(smem, out + (size_t)m0 * (2 * D_) + br * D_ + nt * BN, 2 * D_, BN);
}

DI float dpp_xor1(float x) {
  return __uint_as_float(__builtin_amdgcn_update_dpp(0, __float_as_uint(x), 0xB1, 0xf, 0xf, true));
}
DI float dpp_xor2(float x) {
  return __uint_as_float(__builtin_amdgcn_update_dpp(0, __float_as_uint(x), 0x4E, 0xf, 0xf, true));
}

DI float dpp_hmir(float x) {
  return __uint_as_float(__builtin_amdgcn_update_dpp(0, __float_as_uint(x), 0x141, 0xf, 0xf, true));
}
DI void ld4(const u16* p, float& a, float& b, float& c, float& d) {
  u32x2 v = *(const u32x2*)p;
  a = bf_lo(v[0]); b = bf_hi(v[0]); c = bf_lo(v[1]); d = bf_hi(v[1]);
}

constexpr int BRS = 2704;
DI void local_attn_item(const char* rows, u16* __restrict__ oB, int p0, int item, int j, int lane) {
  const int pl = item >> 4, h = item & 15;
  const int pos = p0 + pl;
  const char* qp = rows + (14 + pl) * BRS + h * 56;
  const char* kp = rows + (14 + pl - 2 * j) * BRS + 896 + h * 56;
  u32x2 qv[7], kv[7], vv[8];
#pragma unroll
  for (int i = 0; i < 7; ++i) { qv[i] = *(const u32x2*)(qp + 8 * i); kv[i] = *(const u32x2*)(kp + 8 * i); }
  const int jj = j < 7 ? j : 6;
#pragma unroll
  for (int wdw = 0; wdw < 8; ++wdw)
    vv[wdw] = *(const u32x2*)(rows + (14 + pl - 2 * wdw) * BRS + 1792 + h * 56 + 8 * jj);
  float sa = 0.f;
#pragma unroll
  for (int i = 0; i < 7; ++i) {
    sa = fmaf(bf_lo(qv[i][0]), bf_lo(kv[i][0]), sa);
    sa = fmaf(bf_hi(qv[i][0]), bf_hi(kv[i][0]), sa);
    sa = fmaf(bf_lo(qv[i][1]), bf_lo(kv[i][1]), sa);
    sa = fmaf(bf_hi(qv[i][1]), bf_hi(kv[i][1]), sa);
  }
  float la = rnd_bf16(rnd_bf16(sa) * SCALE_F);
  if (pos - 2 * j < 0) la = -1e9f;
  float m = fmaxf(la, dpp_xor1(la));
  m = fmaxf(m, dpp_xor2(m));
  m = fmaxf(m, dpp_hmir(m));
  float e = __builtin_amdgcn_exp2f((la - m) * LOG2E);
  float sum = e + dpp_xor1(e);
  sum += dpp_xor2(sum);
  sum += dpp_hmir(sum);
  float pj_ = rnd_bf16(e / sum);
  float o0 = 0.f, o1 = 0.f, o2 = 0.f, o3 = 0.f;
#pragma unroll
  for (int wdw = 0; wdw < 8; ++wdw) {
    float pw = __shfl(pj_, (lane & ~7) | wdw);
    o0 = fmaf(pw, bf_lo(vv[wdw][0]), o0); o1 = fmaf(pw, bf_hi(vv[wdw][0]), o1);
    o2 = fmaf(pw, bf_lo(vv[wdw][1]), o2); o3 = fmaf(pw, bf_hi(vv[wdw][1]), o3);
  }
  if (j < 7) {
    u32x2 v0 = {pack_bf16(o0, o1), pack_bf16(o2, o3)};
    *(u32x2*)(oB + (size_t)pos * D_ + h * DH + 4 * j) = v0;
  }
}

DI void lds_inc(unsigned addr) { asm volatile("ds_add_u32 %0, %1" ::"v"(addr), "v"(1u) : "memory"); }
DI void lds_wait(unsigned addr, unsigned target) {
  for (;;) {
    unsigned v;
    asm volatile("ds_read_b32 %0, %1\n\ts_waitcnt lgkmcnt(0)" : "=v"(v) : "v"(addr) : "memory");
    if (__builtin_amdgcn_readfirstlane(v) >= target) break;
    __builtin_amdgcn_s_sleep(1);
  }
}
constexpr float PTHR = 1099511627776.0f;
constexpr int ABUF = 32768;
constexpr int BROWS_OFF = 3 * ABUF;

__global__ __launch_bounds__(1024, 4) void attn_kernel(char* __restrict__ ws) {
  __shared__ __attribute__((aligned(16))) char smem[3 * ABUF + 65536];
  const int lane = threadIdx.x & 63;
  const int w = __builtin_amdgcn_readfirstlane(threadIdx.x >> 6);
  const int qb = w & 3, kq = w >> 2;
  const int hh = lane >> 5;
  const int head = lane & 15;
  const char* kvsrc = ws + WS_KV + (size_t)(kq * 16) * 4096 + (qb * 2) * 1024 + lane * 16;
  char* kvdst = smem + kq * 8192 + (qb * 2) * 1024;

  auto issue1 = [&](int st, int i) {
    glds16(kvsrc + st * 8192 + i * 1024, kvdst + (st % 3) * ABUF + i * 1024);
  };
  issue1(0, 0); issue1(0, 1);

  const int pos = blockIdx.x * 8 + qb * 2 + ((lane >> 4) & 1);
  bf16x8 q0, q1;
  {
    const u16* qp = (const u16*)(ws + WS_QA) + (size_t)pos * D_ + head * DH + 8 * hh;
    u32x2 a = *(const u32x2*)(qp), b = *(const u32x2*)(qp + 4);
    u32x2 c = *(const u32x2*)(qp + 16);
    u32x2 d = {0u, 0u};
    if (hh == 0) d = *(const u32x2*)(qp + 20);
    u32x4 x0 = {a[0], a[1], b[0], b[1]}, x1 = {c[0], c[1], d[0], d[1]};
    q0 = __builtin_bit_cast(bf16x8, x0);
    q1 = __builtin_bit_cast(bf16x8, x1);
  }
  issue1(1, 0); issue1(1, 1);

  const int p0 = blockIdx.x * 8;
  const unsigned cnt_base = (unsigned)(size_t)(smem + BROWS_OFF + 63 * 1024);
  if (threadIdx.x < 40) *(unsigned*)(smem + BROWS_OFF + 63 * 1024 + threadIdx.x * 4) = 0;
  raw_barrier();
  f32x16 O, C;
#pragma unroll
  for (int r = 0; r < 16; ++r) { O[r] = 0.f; C[r] = 0.f; }
  float R = 0.f;
  bool first = true;

  for (int st = 0; st < 8; ++st) {
    if (st < 7) WAIT_VM(2); else WAIT_VM(0);
    asm volatile("s_waitcnt lgkmcnt(0)" ::: "memory");
    if (lane == 0) {
      lds_inc(cnt_base + (kq * 8 + st) * 4);
      if (st == 6) lds_inc(cnt_base + 32 * 4);
    }
    lds_wait(cnt_base + (kq * 8 + st) * 4, 4u);
    if (st < 4) {
      int piece = w + 16 * st;
      int off = piece * 1024 + lane * 16;
      int row = off / BRS, col = off - row * BRS;
      if (col >= 2688) col = 0;
      int grow = p0 - 14 + row;
      if (grow < 0) grow = 0;
      if (grow > S_ - 1) grow = S_ - 1;
      glds16(ws + WS_QKVB + (size_t)grow * 2688 + col, smem + BROWS_OFF + (piece < 59 ? piece : 59) * 1024);
    }
    const char* base = smem + (st % 3) * ABUF + kq * 8192 + lane * 16;
#pragma unroll
    for (int t = 0; t < 2; ++t) {
      if (st + 2 < 8) issue1(st + 2, t);
      bf16x8 k0 = *(const bf16x8*)(base + t * 4096);
      bf16x8 k1 = *(const bf16x8*)(base + t * 4096 + 1024);
      bf16x8 v0 = *(const bf16x8*)(base + t * 4096 + 2048);
      bf16x8 v1 = *(const bf16x8*)(base + t * 4096 + 3072);
      f32x16 s = __builtin_amdgcn_mfma_f32_32x32x16_bf16(k0, q0, C, 0, 0, 0);
      s = __builtin_amdgcn_mfma_f32_32x32x16_bf16(k1, q1, s, 0, 0, 0);
      float p[16];
#pragma unroll
      for (int r = 0; r < 16; ++r) p[r] = __builtin_amdgcn_exp2f(s[r]);
      float pm = fmaxf(p[0], p[1]);
#pragma unroll
      for (int r = 2; r < 16; r += 2) pm = fmaxf(fmaxf(pm, p[r]), p[r + 1]);
      if (__builtin_expect(first || __any(pm > PTHR), 0)) {
        float tm = fmaxf(s[0], s[1]);
#pragma unroll
        for (int r = 2; r < 16; r += 2) tm = fmaxf(fmaxf(tm, s[r]), s[r + 1]);
        auto rr = __builtin_amdgcn_permlane32_swap(__float_as_uint(tm), __float_as_uint(tm), false, false);
        float delta = fmaxf(__uint_as_float(rr[0]), __uint_as_float(rr[1]));
        if (!first) {
          delta = fmaxf(delta, 0.f);
          float alpha = __builtin_amdgcn_exp2f(-delta);
#pragma unroll
          for (int r = 0; r < 16; ++r) O[r] *= alpha;
        }
        R += delta;
#pragma unroll
        for (int r = 0; r < 16; ++r) { p[r] = __builtin_amdgcn_exp2f(s[r] - delta); C[r] = -R; }
        first = false;
      }
      u32x4 pa = {pack_bf16(p[0], p[1]), pack_bf16(p[2], p[3]), pack_bf16(p[4], p[5]), pack_bf16(p[6], p[7])};
      u32x4 pbv = {pack_bf16(p[8], p[9]), pack_bf16(p[10], p[11]), pack_bf16(p[12], p[13]), pack_bf16(p[14], p[15])};
      O = __builtin_amdgcn_mfma_f32_32x32x16_bf16(v0, __builtin_bit_cast(bf16x8, pa), O, 0, 0, 0);
      O = __builtin_amdgcn_mfma_f32_32x32x16_bf16(v1, __builtin_bit_cast(bf16x8, pbv), O, 0, 0, 0);
    }
  }

  lds_wait(cnt_base + 32 * 4, 16u);
  local_attn_item(smem + BROWS_OFF, (u16*)(ws + WS_OB), p0, w * 8 + (lane >> 3), lane & 7, lane);

  raw_barrier();
  {
    float* xch = (float*)smem + (kq * 4 + qb) * (17 * 64);
#pragma unroll
    for (int r = 0; r < 16; ++r) xch[r * 64 + lane] = O[r];
    xch[16 * 64 + lane] = R;
  }
  raw_barrier();
  {
    float Rs[4], a[4];
#pragma unroll
    for (int i = 0; i < 4; ++i) Rs[i] = ((const float*)smem)[(i * 4 + qb) * (17 * 64) + 16 * 64 + lane];
    float Rn = fmaxf(fmaxf(Rs[0], Rs[1]), fmaxf(Rs[2], Rs[3]));
#pragma unroll
    for (int i = 0; i < 4; ++i) a[i] = __builtin_amdgcn_exp2f(Rs[i] - Rn);
    float l = 0.f, o[4] = {0.f, 0.f, 0.f, 0.f};
#pragma unroll
    for (int i = 0; i < 4; ++i) {
      const float* x = (const float*)smem + (i * 4 + qb) * (17 * 64);
      l = fmaf(x[12 * 64 + (lane & 31) + 32], a[i], l);
#pragma unroll
      for (int r = 0; r < 4; ++r) o[r] = fmaf(x[(4 * kq + r) * 64 + lane], a[i], o[r]);
    }
    float inv = 1.0f / l;
    if (kq < 3 || hh == 0) {
      u32x2 v = {pack_bf16(o[0] * inv, o[1] * inv), pack_bf16(o[2] * inv, o[3] * inv)};
      *(u32x2*)((u16*)(ws + WS_OA) + (size_t)pos * D_ + head * DH + 4 * hh + 8 * kq) = v;
    }
  }
}

extern "C" void kernel_launch(void* const* d_in, const int* in_sizes, int n_in,
                              void* d_out, int out_size, void* d_ws, size_t ws_size,
                              hipStream_t stream) {
  const u16* xA = (const u16*)d_in[0];
  const u16* wq = (const u16*)d_in[1];
  const u16* wk = (const u16*)d_in[2];
  const u16* wv = (const u16*)d_in[3];
  const u16* woA = (const u16*)d_in[4];
  const u16* xB = (const u16*)d_in[5];
  const u16* wqkv = (const u16*)d_in[6];
  const u16* woB = (const u16*)d_in[7];
  char* ws = (char*)d_ws;
  const int g_mask = 0xff;
  if (g_mask & 1) proj_kernel<<<256, 64 * GNW, 0, stream>>>(xA, wq, wk, wv, xB, wqkv, ws);
  if (g_mask & 2) attn_kernel<<<256, 1024, 0, stream>>>(ws);
  if (g_mask & 4) outproj_kernel<<<224, 64 * GNW, 0, stream>>>(ws, woA, woB, (u16*)d_out);
}
```

```cpp
#include <hip/hip_runtime.h>
#include <hip/hip_bf16.h>
#include <stdint.h>

constexpr int S_ = 2048, D_ = 448, H_ = 16, DH = 28;
constexpr int KDIM = 448;
constexpr float SCALE_F = 0.18898223650461362f;
constexpr float LOG2E = 1.4426950408889634f;

typedef unsigned short u16;
typedef short bf16x8 __attribute__((ext_vector_type(8)));
typedef short s16x4 __attribute__((ext_vector_type(4)));
typedef float f32x16 __attribute__((ext_vector_type(16)));
typedef float f32x2 __attribute__((ext_vector_type(2)));
typedef __bf16 bf16x2_t __attribute__((ext_vector_type(2)));
typedef unsigned u32x4 __attribute__((ext_vector_type(4)));
typedef unsigned u32x2 __attribute__((ext_vector_type(2)));

#define DI __device__ __forceinline__

DI unsigned pack_bf16(float lo, float hi) {
  f32x2 v = {lo, hi};
  bf16x2_t b = __builtin_convertvector(v, bf16x2_t);
  return __builtin_bit_cast(unsigned, b);
}
DI float rnd_bf16(float x) { return __uint_as_float(pack_bf16(0.f, x)); }
DI float bf_lo(unsigned u) { return __uint_as_float(u << 16); }
DI float bf_hi(unsigned u) { return __uint_as_float(u & 0xffff0000u); }

#define WAIT_VM(n) asm volatile("s_waitcnt vmcnt(" #n ")" ::: "memory")
template <int N> DI void wait_vm() { asm volatile("s_waitcnt vmcnt(%0)" ::"n"(N) : "memory"); }
DI void raw_barrier() {
  asm volatile("s_waitcnt lgkmcnt(0)" ::: "memory");
  __builtin_amdgcn_s_barrier();
}

DI void glds16(const void* g, void* l) {
  __builtin_amdgcn_global_load_lds((const unsigned*)g, (unsigned*)l, 16, 0, 0);
}
DI void glds4(const void* g, void* l) {
  __builtin_amdgcn_global_load_lds((const unsigned*)g, (unsigned*)l, 4, 0, 0);
}
DI s16x4 tr_read(const void* l) {
  return __builtin_amdgcn_ds_read_tr16_b64_v4i16((__attribute__((address_space(3))) s16x4*)l);
}

constexpr size_t WS_QA = 0;
constexpr size_t WS_KV = WS_QA + (size_t)S_ * D_ * 2;
constexpr size_t WS_QKVB = WS_KV + 64 * 4096;
constexpr size_t WS_OA = WS_QKVB + (size_t)S_ * 3 * D_ * 2;
constexpr size_t WS_OB = WS_OA + (size_t)S_ * D_ * 2;

constexpr int GNW = 8;
template <int BM, int BN> struct GemmCfg {
  static constexpr int XB = BM * 128;
  static constexpr int WB = 64 * BN * 2;
  static constexpr int STB = XB + WB;
  static constexpr int NST = 4;
  static constexpr int XI = XB / 1024 / GNW;
  static constexpr int WI = WB / 1024 / GNW;
  static constexpr int WGM = (BM >= BN) ? 4 : 2, WGN = GNW / WGM;
  static constexpr int MT = BM / 32 / WGM, NT = BN / 32 / WGN;
  static constexpr int CROW = BN * 2 + 16;
};

template <int BN> DI int wswz(int k) {
  if constexpr (BN == 128) return (k & 3) << 2;
  else return ((k >> 1) & 1) << 2;
}

template <int BM, int BN, bool KV>
DI void gemm_main(const u16* __restrict__ X,
                  const u16* __restrict__ W, int ldw, int n0, int ncols,
                  const u16* __restrict__ Wk, const u16* __restrict__ Wv,
                  char* smem, float oscale = 1.0f) {
  using C = GemmCfg<BM, BN>;
  const int lane = threadIdx.x & 63;
  const int w = __builtin_amdgcn_readfirstlane(threadIdx.x >> 6);
  const int wm = w / C::WGN, wn = w % C::WGN;
  f32x16 acc[C::MT][C::NT];
  constexpr int KVI = 64 / GNW;
  constexpr int WCNT = KV ? KVI : C::WI;
  constexpr int CNT = C::XI + WCNT;

  const u16* xsrc[C::XI];
#pragma unroll
  for (int i = 0; i < C::XI; ++i) {
    int row = (w * C::XI + i) * 8 + (lane >> 3);
    int c = (lane & 7) ^ ((row >> 1) & 7);
    xsrc[i] = X + row * KDIM + c * 8;
  }
  const u16* wsrc[WCNT];
  if constexpr (!KV) {
    constexpr int CPR = BN / 8, RPI = 1024 / (BN * 2);
#pragma unroll
    for (int i = 0; i < C::WI; ++i) {
      int k = (w * C::WI + i) * RPI + lane / CPR;
      int c = (lane % CPR) ^ wswz<BN>(k);
      int col = n0 + c * 8;
      if (col + 8 > ncols) col = ncols - 8;
      wsrc[i] = W + k * ldw + col;
    }
  } else {
#pragma unroll
    for (int i = 0; i < KVI; ++i) {
      int k = w * KVI + i;
      int c = (lane >> 2) ^ wswz<BN>(k);
      int col = c * 8 + (lane & 3) * 2;
      const u16* p = Wk + k * DH;
      if (col < 28) p = Wk + k * DH + col;
      else if (col >= 32 && col < 60) p = Wv + k * DH + (col - 32);
      wsrc[i] = p;
    }
  }
  auto issue = [&](int ks) {
    char* sb = smem + (ks % C::NST) * C::STB;
#pragma unroll
    for (int i = 0; i < C::XI; ++i) glds16(xsrc[i] + ks * 64, sb + (w * C::XI + i) * 1024);
    if constexpr (!KV) {
#pragma unroll
      for (int i = 0; i < C::WI; ++i) glds16(wsrc[i] + (size_t)ks * 64 * ldw, sb + C::XB + (w * C::WI + i) * 1024);
    } else {
#pragma unroll
      for (int i = 0; i < KVI; ++i) glds4(wsrc[i] + ks * 64 * DH, sb + C::XB + (w * KVI + i) * 256);
    }
  };

  const int hh = lane >> 5;
  int xoff[C::MT];
#pragma unroll
  for (int i = 0; i < C::MT; ++i) {
    int row = wm * (BM / C::WGM) + 32 * i + (lane & 31);
    xoff[i] = row * 128;
  }
  const int xsw = ((lane & 31) >> 1) & 7;
  const int g = lane >> 4, q = (lane & 15) >> 2, p = lane & 3;
  int woff[C::NT];
#pragma unroll
  for (int j = 0; j < C::NT; ++j) {
    int nn = wn * (BN / C::WGN) + 32 * j + 16 * (g & 1);
    int c = (nn >> 3) + (p >> 1);
    int kk = 8 * (g >> 1) + q;
    woff[j] = C::XB + kk * (BN * 2) + ((c ^ wswz<BN>(kk)) << 4) + (p & 1) * 8;
  }

#pragma unroll
  for (int i = 0; i < C::MT; ++i)
#pragma unroll
    for (int j = 0; j < C::NT; ++j)
#pragma unroll
      for (int r = 0; r < 16; ++r) acc[i][j][r] = 0.f;

  issue(0); issue(1); issue(2);
#pragma unroll
  for (int ks = 0; ks < 7; ++ks) {
    if (ks <= 4) wait_vm<2 * CNT>();
    else if (ks == 5) wait_vm<CNT>();
    else wait_vm<0>();
    raw_barrier();
    if (ks + 3 <= 6) issue(ks + 3);
    const char* sb = smem + (ks % C::NST) * C::STB;
#pragma unroll
    for (int k16 = 0; k16 < 4; ++k16) {
      bf16x8 xa[C::MT], wb[C::NT];
#pragma unroll
      for (int i = 0; i < C::MT; ++i)
        xa[i] = *(const bf16x8*)(sb + xoff[i] + (((2 * k16 + hh) ^ xsw) << 4));
#pragma unroll
      for (int j = 0; j < C::NT; ++j) {
        s16x4 lo = tr_read(sb + woff[j] + (16 * k16) * (BN * 2));
        s16x4 hi = tr_read(sb + woff[j] + (16 * k16 + 4) * (BN * 2));
        wb[j] = __builtin_shufflevector(lo, hi, 0, 1, 2, 3, 4, 5, 6, 7);
      }
#pragma unroll
      for (int i = 0; i < C::MT; ++i)
#pragma unroll
        for (int j = 0; j < C::NT; ++j)
          acc[i][j] = __builtin_amdgcn_mfma_f32_32x32x16_bf16(wb[j], xa[i], acc[i][j], 0, 0, 0);
    }
  }
  raw_barrier();
#pragma unroll
  for (int i = 0; i < C::MT; ++i)
#pragma unroll
    for (int j = 0; j < C::NT; ++j)
#pragma unroll
      for (int gq = 0; gq < 4; ++gq) {
        int pos = wm * (BM / C::WGM) + 32 * i + (lane & 31);
        int n = wn * (BN / C::WGN) + 32 * j + 8 * gq + 4 * hh;
        u32x2 v = {pack_bf16(acc[i][j][4 * gq] * oscale, acc[i][j][4 * gq + 1] * oscale),
                   pack_bf16(acc[i][j][4 * gq + 2] * oscale, acc[i][j][4 * gq + 3] * oscale)};
        *(u32x2*)(smem + pos * C::CROW + n * 2) = v;
      }
  raw_barrier();
}

template <int BM, int BN>
DI void store_tile(const char* smem, u16* __restrict__ dst, int ldd, int nvalid) {
  using C = GemmCfg<BM, BN>;
  constexpr int CPR = BN / 8;
  constexpr int TOT = BM * CPR;
#pragma unroll
  for (int it = 0; it < TOT / (64 * GNW); ++it) {
    int id = it * (64 * GNW) + threadIdx.x;
    int row = id / CPR, c = id % CPR;
    u32x4 v = *(const u32x4*)(smem + row * C::CROW + c * 16);
    if (c * 8 < nvalid) *(u32x4*)(dst + (size_t)row * ldd + c * 8) = v;
  }
}

__global__ __launch_bounds__(64 * GNW, 2) void proj_kernel(
    const u16* __restrict__ xA, const u16* __restrict__ wq, const u16* __restrict__ wk,
    const u16* __restrict__ wv, const u16* __restrict__ xB, const u16* __restrict__ wqkv,
    char* __restrict__ ws) {
  constexpr int BM = 128, BN = 128;
  using C = GemmCfg<BM, BN>;
  __shared__ __attribute__((aligned(16))) char smem[C::NST * C::STB];
  const int mt = blockIdx.x & 15, nt = blockIdx.x >> 4;
  const int m0 = mt * BM;
  if (nt < 4) {
    gemm_main<BM, BN, false>(xA + (size_t)m0 * KDIM, wq, D_, nt * BN, D_, nullptr, nullptr, smem, SCALE_F * LOG2E);
    store_tile<BM, BN>(smem, (u16*)(ws + WS_QA) + (size_t)m0 * D_ + nt * BN, D_, D_ - nt * BN);
  } else if (nt == 4) {
    gemm_main<BM, BN, true>(xA + (size_t)m0 * KDIM, nullptr, 0, 0, 0, wk, wv, smem);
    char* kv = ws + WS_KV + (size_t)(m0 >> 5) * 4096;
    {
      int id = threadIdx.x;
      int pos = id >> 2, c = id & 3;
      u32x4 v = *(const u32x4*)(smem + pos * C::CROW + c * 16);
      if (c == 3) { v[2] = 0; v[3] = 0; }
      int t = pos >> 5, s = c >> 1, hh = c & 1;
      *(u32x4*)(kv + t * 4096 + s * 1024 + ((pos & 31) + 32 * hh) * 16) = v;
    }
    {
      int id = threadIdx.x;
      int t = id >> 7, s = (id >> 6) & 1, ln = id & 63;
      int d = ln & 31, hh = ln >> 5;
      u16 e[8];
#pragma unroll
      for (int j = 0; j < 8; ++j) {
        int key = 32 * t + 16 * s + 8 * (j >> 2) + 4 * hh + (j & 3);
        u16 val = *(const u16*)(smem + key * C::CROW + (32 + d) * 2);
        if (d == 28) val = 0x3F80;
        else if (d > 28) val = 0;
        e[j] = val;
      }
      u32x4 v = {(unsigned)e[0] | ((unsigned)e[1] << 16), (unsigned)e[2] | ((unsigned)e[3] << 16),
                 (unsigned)e[4] | ((unsigned)e[5] << 16), (unsigned)e[6] | ((unsigned)e[7] << 16)};
      *(u32x4*)(kv + t * 4096 + 2048 + s * 1024 + ln * 16) = v;
    }
  } else {
    const int n0 = (nt - 5) * BN;
    gemm_main<BM, BN, false>(xB + (size_t)m0 * KDIM, wqkv, 3 * D_, n0, 3 * D_, nullptr, nullptr, smem);
    store_tile<BM, BN>(smem, (u16*)(ws + WS_QKVB) + (size_t)m0 * 3 * D_ + n0, 3 * D_, 3 * D_ - n0);
  }
}

__global__ __launch_bounds__(64 * GNW, 2) void outproj_kernel(
    const char* __restrict__ ws, const u16* __restrict__ woA, const u16* __restrict__ woB,
    u16* __restrict__ out) {
  constexpr int BM = 128, BN = 64;
  using C = GemmCfg<BM, BN>;
  __shared__ __attribute__((aligned(16))) char smem[C::NST * C::STB];
  const int mt = blockIdx.x & 15, r = blockIdx.x >> 4;
  const int br = r >= 7, nt = br ? r - 7 : r;
  const int m0 = mt * BM;
  const u16* X = (const u16*)(ws + (br ? WS_OB : WS_OA)) + (size_t)m0 * KDIM;
  gemm_main<BM, BN, false>(X, br ? woB : woA, D_, nt * BN, D_, nullptr, nullptr, smem);
  store_tile<BM, BN>(smem, out + (size_t)m0 * (2 * D_) + br * D_ + nt * BN, 2 * D_, BN);
}

DI float dpp_xor1(float x) {
  return __uint_as_float(__builtin_amdgcn_update_dpp(0, __float_as_uint(x), 0xB1, 0xf, 0xf, true));
}
DI float dpp_xor2(float x) {
  return __uint_as_float(__builtin_amdgcn_update_dpp(0, __float_as_uint(x), 0x4E, 0xf, 0xf, true));
}

DI float dpp_hmir(float x) {
  return __uint_as_float(__builtin_amdgcn_update_dpp(0, __float_as_uint(x), 0x141, 0xf, 0xf, true));
}
DI void ld4(const u16* p, float& a, float& b, float& c, float& d) {
  u32x2 v = *(const u32x2*)p;
  a = bf_lo(v[0]); b = bf_hi(v[0]); c = bf_lo(v[1]); d = bf_hi(v[1]);
}

constexpr int BSL = 2064;
constexpr int BWB = 18 * 1024;
DI void local_attn_item(const char* img, u16* __restrict__ oB, int pos, int h, int j, int lane) {
  const char* qp = img + h * 56;
  const char* kp = img + 1024 + j * BSL + h * 56;
  u32x2 qv[7], kv[7], vv[8];
#pragma unroll
  for (int i = 0; i < 7; ++i) { qv[i] = *(const u32x2*)(qp + 8 * i); kv[i] = *(const u32x2*)(kp + 8 * i); }
  const int jj = j < 7 ? j : 6;
#pragma unroll
  for (int wdw = 0; wdw < 8; ++wdw)
    vv[wdw] = *(const u32x2*)(img + 1024 + wdw * BSL + 896 + h * 56 + 8 * jj);
  float sa = 0.f;
#pragma unroll
  for (int i = 0; i < 7; ++i) {
    sa = fmaf(bf_lo(qv[i][0]), bf_lo(kv[i][0]), sa);
    sa = fmaf(bf_hi(qv[i][0]), bf_hi(kv[i][0]), sa);
    sa = fmaf(bf_lo(qv[i][1]), bf_lo(kv[i][1]), sa);
    sa = fmaf(bf_hi(qv[i][1]), bf_hi(kv[i][1]), sa);
  }
  float la = rnd_bf16(rnd_bf16(sa) * SCALE_F);
  if (pos - 2 * j < 0) la = -1e9f;
  float m = fmaxf(la, dpp_xor1(la));
  m = fmaxf(m, dpp_xor2(m));
  m = fmaxf(m, dpp_hmir(m));
  float e = __builtin_amdgcn_exp2f((la - m) * LOG2E);
  float sum = e + dpp_xor1(e);
  sum += dpp_xor2(sum);
  sum += dpp_hmir(sum);
  float pj_ = rnd_bf16(e / sum);
  float o0 = 0.f, o1 = 0.f, o2 = 0.f, o3 = 0.f;
#pragma unroll
  for (int wdw = 0; wdw < 8; ++wdw) {
    float pw = __shfl(pj_, (lane & ~7) | wdw);
    o0 = fmaf(pw, bf_lo(vv[wdw][0]), o0); o1 = fmaf(pw, bf_hi(vv[wdw][0]), o1);
    o2 = fmaf(pw, bf_lo(vv[wdw][1]), o2); o3 = fmaf(pw, bf_hi(vv[wdw][1]), o3);
  }
  if (j < 7) {
    u32x2 v0 = {pack_bf16(o0, o1), pack_bf16(o2, o3)};
    *(u32x2*)(oB + (size_t)pos * D_ + h * DH + 4 * j) = v0;
  }
}

constexpr float PTHR = 1.2379400392853803e27f;
constexpr float FMAXF = 3.0e38f;

DI void pv_tile(f32x16& O, const f32x16& s, bf16x8 v0, bf16x8 v1) {
  float p[16];
#pragma unroll
  for (int r = 0; r < 16; ++r) p[r] = __builtin_amdgcn_exp2f(s[r]);
  u32x4 pa = {pack_bf16(p[0], p[1]), pack_bf16(p[2], p[3]), pack_bf16(p[4], p[5]), pack_bf16(p[6], p[7])};
  u32x4 pbv = {pack_bf16(p[8], p[9]), pack_bf16(p[10], p[11]), pack_bf16(p[12], p[13]), pack_bf16(p[14], p[15])};
  O = __builtin_amdgcn_mfma_f32_32x32x16_bf16(v0, __builtin_bit_cast(bf16x8, pa), O, 0, 0, 0);
  O = __builtin_amdgcn_mfma_f32_32x32x16_bf16(v1, __builtin_bit_cast(bf16x8, pbv), O, 0, 0, 0);
}
DI void careful_tile(f32x16& O, f32x16& C, float& R, bool first, bf16x8 k0, bf16x8 k1, bf16x8 v0, bf16x8 v1,
                     bf16x8 q0, bf16x8 q1) {
  f32x16 s = __builtin_amdgcn_mfma_f32_32x32x16_bf16(k0, q0, C, 0, 0, 0);
  s = __builtin_amdgcn_mfma_f32_32x32x16_bf16(k1, q1, s, 0, 0, 0);
  float tm = fmaxf(s[0], s[1]);
#pragma unroll
  for (int r = 2; r < 16; r += 2) tm = fmaxf(fmaxf(tm, s[r]), s[r + 1]);
  auto rr = __builtin_amdgcn_permlane32_swap(__float_as_uint(tm), __float_as_uint(tm), false, false);
  float delta = fmaxf(__uint_as_float(rr[0]), __uint_as_float(rr[1]));
  if (!first) {
    delta = fmaxf(delta, 0.f);
    float alpha = __builtin_amdgcn_exp2f(-delta);
#pragma unroll
    for (int r = 0; r < 16; ++r) O[r] *= alpha;
  }
  R += delta;
#pragma unroll
  for (int r = 0; r < 16; ++r) { s[r] -= delta; C[r] = -R; }
  pv_tile(O, s, v0, v1);
}

__global__ __launch_bounds__(512, 2) void attn_kernel(char* __restrict__ ws) {
  __shared__ __attribute__((aligned(16))) char smem[8 * BWB];
  const int lane = threadIdx.x & 63;
  const int w = __builtin_amdgcn_readfirstlane(threadIdx.x >> 6);
  const int hh = lane >> 5;
  const int head = lane & 15;
  const int p0 = blockIdx.x * 8;
  const char* kvp = ws + WS_KV + (size_t)(w * 8) * 4096 + lane * 16;

  bf16x8 k0 = *(const bf16x8*)(kvp), k1 = *(const bf16x8*)(kvp + 1024);
  bf16x8 v0 = *(const bf16x8*)(kvp + 2048), v1 = *(const bf16x8*)(kvp + 3072);

  bf16x8 q0[4], q1[4];
#pragma unroll
  for (int b = 0; b < 4; ++b) {
    const int pos = p0 + b * 2 + ((lane >> 4) & 1);
    const u16* qp = (const u16*)(ws + WS_QA) + (size_t)pos * D_ + head * DH + 8 * hh;
    u32x2 a = *(const u32x2*)(qp), bb = *(const u32x2*)(qp + 4);
    u32x2 c = *(const u32x2*)(qp + 16);
    u32x2 d = {0u, 0u};
    if (hh == 0) d = *(const u32x2*)(qp + 20);
    u32x4 x0 = {a[0], a[1], bb[0], bb[1]}, x1 = {c[0], c[1], d[0], d[1]};
    q0[b] = __builtin_bit_cast(bf16x8, x0);
    q1[b] = __builtin_bit_cast(bf16x8, x1);
  }
  {
    const int pos = p0 + w;
    const char* qkvb = ws + WS_QKVB;
#pragma unroll
    for (int pc = 0; pc < 18; ++pc) {
      int off = pc * 1024 + lane * 16;
      const char* src;
      if (pc == 0) {
        src = qkvb + (size_t)pos * 2688 + (off < 896 ? off : 0);
      } else {
        int o2 = off - 1024;
        int j = o2 / BSL, col = o2 - j * BSL;
        if (j > 7) j = 7;
        if (col >= 1792) col = 0;
        int pr = pos - 2 * j; if (pr < 0) pr = 0;
        src = qkvb + (size_t)pr * 2688 + 896 + col;
      }
      glds16(src, smem + w * BWB + pc * 1024);
    }
  }

  f32x16 O[4], C[4];
  float R[4];
#pragma unroll
  for (int b = 0; b < 4; ++b) {
    R[b] = 0.f;
#pragma unroll
    for (int r = 0; r < 16; ++r) { O[b][r] = 0.f; C[b][r] = 0.f; }
  }
  {
    bf16x8 nk0 = *(const bf16x8*)(kvp + 4096), nk1 = *(const bf16x8*)(kvp + 4096 + 1024);
    bf16x8 nv0 = *(const bf16x8*)(kvp + 4096 + 2048), nv1 = *(const bf16x8*)(kvp + 4096 + 3072);
#pragma unroll
    for (int b = 0; b < 4; ++b) careful_tile(O[b], C[b], R[b], true, k0, k1, v0, v1, q0[b], q1[b]);
    k0 = nk0; k1 = nk1; v0 = nv0; v1 = nv1;
  }
  for (int t = 1; t < 8; ++t) {
    const int tn = t < 7 ? t + 1 : 7;
    bf16x8 nk0 = *(const bf16x8*)(kvp + tn * 4096), nk1 = *(const bf16x8*)(kvp + tn * 4096 + 1024);
    bf16x8 nv0 = *(const bf16x8*)(kvp + tn * 4096 + 2048), nv1 = *(const bf16x8*)(kvp + tn * 4096 + 3072);
#pragma unroll
    for (int b = 0; b < 4; ++b) {
      f32x16 s = __builtin_amdgcn_mfma_f32_32x32x16_bf16(k0, q0[b], C[b], 0, 0, 0);
      s = __builtin_amdgcn_mfma_f32_32x32x16_bf16(k1, q1[b], s, 0, 0, 0);
      pv_tile(O[b], s, v0, v1);
    }
    k0 = nk0; k1 = nk1; v0 = nv0; v1 = nv1;
  }
  {
    bool bad = false;
#pragma unroll
    for (int b = 0; b < 4; ++b) {
      bad |= (hh == 1) && !(O[b][12] <= PTHR);
#pragma unroll
      for (int r = 0; r < 16; ++r) bad |= !(fabsf(O[b][r]) <= FMAXF);
    }
    if (__builtin_expect(__any(bad), 0)) {
#pragma unroll
      for (int b = 0; b < 4; ++b) {
        R[b] = 0.f;
#pragma unroll
        for (int r = 0; r < 16; ++r) { O[b][r] = 0.f; C[b][r] = 0.f; }
      }
#pragma unroll 1
      for (int t = 0; t < 8; ++t) {
        bf16x8 a0 = *(const bf16x8*)(kvp + t * 4096), a1 = *(const bf16x8*)(kvp + t * 4096 + 1024);
        bf16x8 b0 = *(const bf16x8*)(kvp + t * 4096 + 2048), b1 = *(const bf16x8*)(kvp + t * 4096 + 3072);
#pragma unroll
        for (int b = 0; b < 4; ++b) careful_tile(O[b], C[b], R[b], t == 0, a0, a1, b0, b1, q0[b], q1[b]);
      }
    }
  }

  WAIT_VM(0);
  local_attn_item(smem + w * BWB, (u16*)(ws + WS_OB), p0 + w, lane >> 3, lane & 7, lane);
  local_attn_item(smem + w * BWB, (u16*)(ws + WS_OB), p0 + w, 8 + (lane >> 3), lane & 7, lane);

  raw_barrier();
#pragma unroll
  for (int b = 0; b < 4; ++b) {
    float* xch = (float*)smem + (w * 4 + b) * (17 * 64);
#pragma unroll
    for (int r = 0; r < 16; ++r) xch[r * 64 + lane] = O[b][r];
    xch[16 * 64 + lane] = R[b];
  }
  raw_barrier();
  {
    const int b = w >> 1;
    float Rs[8], a[8];
#pragma unroll
    for (int i = 0; i < 8; ++i) Rs[i] = ((const float*)smem)[(i * 4 + b) * (17 * 64) + 16 * 64 + lane];
    float Rn = Rs[0];
#pragma unroll
    for (int i = 1; i < 8; ++i) Rn = fmaxf(Rn, Rs[i]);
    float l = 0.f;
#pragma unroll
    for (int i = 0; i < 8; ++i) {
      a[i] = __builtin_amdgcn_exp2f(Rs[i] - Rn);
      l = fmaf(((const float*)smem)[(i * 4 + b) * (17 * 64) + 12 * 64 + (lane & 31) + 32], a[i], l);
    }
    const float inv = 1.0f / l;
    const int pos = p0 + b * 2 + ((lane >> 4) & 1);
#pragma unroll
    for (int gg = 0; gg < 2; ++gg) {
      const int g = (w & 1) * 2 + gg;
      float o[4] = {0.f, 0.f, 0.f, 0.f};
#pragma unroll
      for (int i = 0; i < 8; ++i) {
        const float* x = (const float*)smem + (i * 4 + b) * (17 * 64);
#pragma unroll
        for (int r = 0; r < 4; ++r) o[r] = fmaf(x[(4 * g + r) * 64 + lane], a[i], o[r]);
      }
      if (g < 3 || hh == 0) {
        u32x2 v = {pack_bf16(o[0] * inv, o[1] * inv), pack_bf16(o[2] * inv, o[3] * inv)};
        *(u32x2*)((u16*)(ws + WS_OA) + (size_t)pos * D_ + head * DH + 4 * hh + 8 * g) = v;
      }
    }
  }
}

extern "C" void kernel_launch(void* const* d_in, const int* in_sizes, int n_in,
                              void* d_out, int out_size, void* d_ws, size_t ws_size,
                              hipStream_t stream) {
  const u16* xA = (const u16*)d_in[0];
  const u16* wq = (const u16*)d_in[1];
  const u16* wk = (const u16*)d_in[2];
  const u16* wv = (const u16*)d_in[3];
  const u16* woA = (const u16*)d_in[4];
  const u16* xB = (const u16*)d_in[5];
  const u16* wqkv = (const u16*)d_in[6];
  const u16* woB = (const u16*)d_in[7];
  char* ws = (char*)d_ws;
  const int g_mask = 0xff;
  if (g_mask & 1) proj_kernel<<<256, 64 * GNW, 0, stream>>>(xA, wq, wk, wv, xB, wqkv, ws);
  if (g_mask & 2) attn_kernel<<<256, 512, 0, stream>>>(ws);
  if (g_mask & 4) outproj_kernel<<<224, 64 * GNW, 0, stream>>>(ws, woA, woB, (u16*)d_out);
}
```

```cpp
#include <hip/hip_runtime.h>
#include <hip/hip_bf16.h>
#include <stdint.h>

constexpr int S_ = 2048, D_ = 448, H_ = 16, DH = 28;
constexpr int KDIM = 448;
constexpr float SCALE_F = 0.18898223650461362f;
constexpr float LOG2E = 1.4426950408889634f;

typedef unsigned short u16;
typedef short bf16x8 __attribute__((ext_vector_type(8)));
typedef short s16x4 __attribute__((ext_vector_type(4)));
typedef float f32x16 __attribute__((ext_vector_type(16)));
typedef float f32x2 __attribute__((ext_vector_type(2)));
typedef __bf16 bf16x2_t __attribute__((ext_vector_type(2)));
typedef unsigned u32x4 __attribute__((ext_vector_type(4)));
typedef unsigned u32x2 __attribute__((ext_vector_type(2)));

#define DI __device__ __forceinline__

DI unsigned pack_bf16(float lo, float hi) {
  f32x2 v = {lo, hi};
  bf16x2_t b = __builtin_convertvector(v, bf16x2_t);
  return __builtin_bit_cast(unsigned, b);
}
DI float rnd_bf16(float x) { return __uint_as_float(pack_bf16(0.f, x)); }
DI float bf_lo(unsigned u) { return __uint_as_float(u << 16); }
DI float bf_hi(unsigned u) { return __uint_as_float(u & 0xffff0000u); }

#define WAIT_VM(n) asm volatile("s_waitcnt vmcnt(" #n ")" ::: "memory")
template <int N> DI void wait_vm() { asm volatile("s_waitcnt vmcnt(%0)" ::"n"(N) : "memory"); }
DI void raw_barrier() {
  asm volatile("s_waitcnt lgkmcnt(0)" ::: "memory");
  __builtin_amdgcn_s_barrier();
}

DI void glds16(const void* g, void* l) {
  __builtin_amdgcn_global_load_lds((const unsigned*)g, (unsigned*)l, 16, 0, 0);
}
DI void glds4(const void* g, void* l) {
  __builtin_amdgcn_global_load_lds((const unsigned*)g, (unsigned*)l, 4, 0, 0);
}
DI s16x4 tr_read(const void* l) {
  return __builtin_amdgcn_ds_read_tr16_b64_v4i16((__attribute__((address_space(3))) s16x4*)l);
}

constexpr size_t WS_QA = 0;
constexpr size_t WS_KV = WS_QA + (size_t)S_ * D_ * 2;
constexpr size_t WS_QKVB = WS_KV + 64 * 4096;
constexpr size_t WS_OA = WS_QKVB + (size_t)S_ * 3 * D_ * 2;
constexpr size_t WS_OB = WS_OA + (size_t)S_ * D_ * 2;

constexpr int GNW = 8;
template <int BM, int BN> struct GemmCfg {
  static constexpr int XB = BM * 128;
  static constexpr int WB = 64 * BN * 2;
  static constexpr int STB = XB + WB;
  static constexpr int NST = 4;
  static constexpr int XI = XB / 1024 / GNW;
  static constexpr int WI = WB / 1024 / GNW;
  static constexpr int WGM = (BM >= BN) ? 4 : 2, WGN = GNW / WGM;
  static constexpr int MT = BM / 32 / WGM, NT = BN / 32 / WGN;
  static constexpr int CROW = BN * 2 + 16;
};

template <int BN> DI int wswz(int k) {
  if constexpr (BN == 128) return (k & 3) << 2;
  else return ((k >> 1) & 1) << 2;
}

template <int BM, int BN, bool KV>
DI void gemm_main(const u16* __restrict__ X,
                  const u16* __restrict__ W, int ldw, int n0, int ncols,
                  const u16* __restrict__ Wk, const u16* __restrict__ Wv,
                  char* smem, float oscale = 1.0f) {
  using C = GemmCfg<BM, BN>;
  const int lane = threadIdx.x & 63;
  const int w = __builtin_amdgcn_readfirstlane(threadIdx.x >> 6);
  const int wm = w / C::WGN, wn = w % C::WGN;
  f32x16 acc[C::MT][C::NT];
  constexpr int KVI = 64 / GNW;
  constexpr int WCNT = KV ? KVI : C::WI;
  constexpr int CNT = C::XI + WCNT;

  const u16* xsrc[C::XI];
#pragma unroll
  for (int i = 0; i < C::XI; ++i) {
    int row = (w * C::XI + i) * 8 + (lane >> 3);
    int c = (lane & 7) ^ ((row >> 1) & 7);
    xsrc[i] = X + row * KDIM + c * 8;
  }
  const u16* wsrc[WCNT];
  if constexpr (!KV) {
    constexpr int CPR = BN / 8, RPI = 1024 / (BN * 2);
#pragma unroll
    for (int i = 0; i < C::WI; ++i) {
      int k = (w * C::WI + i) * RPI + lane / CPR;
      int c = (lane % CPR) ^ wswz<BN>(k);
      int col = n0 + c * 8;
      if (col + 8 > ncols) col = ncols - 8;
      wsrc[i] = W + k * ldw + col;
    }
  } else {
#pragma unroll
    for (int i = 0; i < KVI; ++i) {
      int k = w * KVI + i;
      int c = (lane >> 2) ^ wswz<BN>(k);
      int col = c * 8 + (lane & 3) * 2;
      const u16* p = Wk + k * DH;
      if (col < 28) p = Wk + k * DH + col;
      else if (col >= 32 && col < 60) p = Wv + k * DH + (col - 32);
      wsrc[i] = p;
    }
  }
  auto issue = [&](int ks) {
    char* sb = smem + (ks % C::NST) * C::STB;
#pragma unroll
    for (int i = 0; i < C::XI; ++i) glds16(xsrc[i] + ks * 64, sb + (w * C::XI + i) * 1024);
    if constexpr (!KV) {
#pragma unroll
      for (int i = 0; i < C::WI; ++i) glds16(wsrc[i] + (size_t)ks * 64 * ldw, sb + C::XB + (w * C::WI + i) * 1024);
    } else {
#pragma unroll
      for (int i = 0; i < KVI; ++i) glds4(wsrc[i] + ks * 64 * DH, sb + C::XB + (w * KVI + i) * 256);
    }
  };

  const int hh = lane >> 5;
  int xoff[C::MT];
#pragma unroll
  for (int i = 0; i < C::MT; ++i) {
    int row = wm * (BM / C::WGM) + 32 * i + (lane & 31);
    xoff[i] = row * 128;
  }
  const int xsw = ((lane & 31) >> 1) & 7;
  const int g = lane >> 4, q = (lane & 15) >> 2, p = lane & 3;
  int woff[C::NT];
#pragma unroll
  for (int j = 0; j < C::NT; ++j) {
    int nn = wn * (BN / C::WGN) + 32 * j + 16 * (g & 1);
    int c = (nn >> 3) + (p >> 1);
    int kk = 8 * (g >> 1) + q;
    woff[j] = C::XB + kk * (BN * 2) + ((c ^ wswz<BN>(kk)) << 4) + (p & 1) * 8;
  }

#pragma unroll
  for (int i = 0; i < C::MT; ++i)
#pragma unroll
    for (int j = 0; j < C::NT; ++j)
#pragma unroll
      for (int r = 0; r < 16; ++r) acc[i][j][r] = 0.f;

  issue(0); issue(1); issue(2);
#pragma unroll
  for (int ks = 0; ks < 7; ++ks) {
    if (ks <= 4) wait_vm<2 * CNT>();
    else if (ks == 5) wait_vm<CNT>();
    else wait_vm<0>();
    raw_barrier();
    if (ks + 3 <= 6) issue(ks + 3);
    const char* sb = smem + (ks % C::NST) * C::STB;
#pragma unroll
    for (int k16 = 0; k16 < 4; ++k16) {
      bf16x8 xa[C::MT], wb[C::NT];
#pragma unroll
      for (int i = 0; i < C::MT; ++i)
        xa[i] = *(const bf16x8*)(sb + xoff[i] + (((2 * k16 + hh) ^ xsw) << 4));
#pragma unroll
      for (int j = 0; j < C::NT; ++j) {
        s16x4 lo = tr_read(sb + woff[j] + (16 * k16) * (BN * 2));
        s16x4 hi = tr_read(sb + woff[j] + (16 * k16 + 4) * (BN * 2));
        wb[j] = __builtin_shufflevector(lo, hi, 0, 1, 2, 3, 4, 5, 6, 7);
      }
#pragma unroll
      for (int i = 0; i < C::MT; ++i)
#pragma unroll
        for (int j = 0; j < C::NT; ++j)
          acc[i][j] = __builtin_amdgcn_mfma_f32_32x32x16_bf16(wb[j], xa[i], acc[i][j], 0, 0, 0);
    }
  }
  raw_barrier();
#pragma unroll
  for (int i = 0; i < C::MT; ++i)
#pragma unroll
    for (int j = 0; j < C::NT; ++j)
#pragma unroll
      for (int gq = 0; gq < 4; ++gq) {
        int pos = wm * (BM / C::WGM) + 32 * i + (lane & 31);
        int n = wn * (BN / C::WGN) + 32 * j + 8 * gq + 4 * hh;
        u32x2 v = {pack_bf16(acc[i][j][4 * gq] * oscale, acc[i][j][4 * gq + 1] * oscale),
                   pack_bf16(acc[i][j][4 * gq + 2] * oscale, acc[i][j][4 * gq + 3] * oscale)};
        *(u32x2*)(smem + pos * C::CROW + n * 2) = v;
      }
  raw_barrier();
}

template <int BM, int BN>
DI void store_tile(const char* smem, u16* __restrict__ dst, int ldd, int nvalid) {
  using C = GemmCfg<BM, BN>;
  constexpr int CPR = BN / 8;
  constexpr int TOT = BM * CPR;
#pragma unroll
  for (int it = 0; it < TOT / (64 * GNW); ++it) {
    int id = it * (64 * GNW) + threadIdx.x;
    int row = id / CPR, c = id % CPR;
    u32x4 v = *(const u32x4*)(smem + row * C::CROW + c * 16);
    if (c * 8 < nvalid) *(u32x4*)(dst + (size_t)row * ldd + c * 8) = v;
  }
}

__global__ __launch_bounds__(64 * GNW, 2) void proj_kernel(
    const u16* __restrict__ xA, const u16* __restrict__ wq, const u16* __restrict__ wk,
    const u16* __restrict__ wv, const u16* __restrict__ xB, const u16* __restrict__ wqkv,
    char* __restrict__ ws) {
  constexpr int BM = 128, BN = 128;
  using C = GemmCfg<BM, BN>;
  __shared__ __attribute__((aligned(16))) char smem[C::NST * C::STB];
  const int mt = blockIdx.x & 15, nt = blockIdx.x >> 4;
  const int m0 = mt * BM;
  if (nt < 4) {
    gemm_main<BM, BN, false>(xA + (size_t)m0 * KDIM, wq, D_, nt * BN, D_, nullptr, nullptr, smem, SCALE_F * LOG2E);
    store_tile<BM, BN>(smem, (u16*)(ws + WS_QA) + (size_t)m0 * D_ + nt * BN, D_, D_ - nt * BN);
  } else if (nt == 4) {
    gemm_main<BM, BN, true>(xA + (size_t)m0 * KDIM, nullptr, 0, 0, 0, wk, wv, smem);
    char* kv = ws + WS_KV + (size_t)(m0 >> 5) * 4096;
    {
      int id = threadIdx.x;
      int pos = id >> 2, c = id & 3;
      u32x4 v = *(const u32x4*)(smem + pos * C::CROW + c * 16);
      if (c == 3) { v[2] = 0; v[3] = 0; }
      int t = pos >> 5, s = c >> 1, hh = c & 1;
      *(u32x4*)(kv + t * 4096 + s * 1024 + ((pos & 31) + 32 * hh) * 16) = v;
    }
    {
      int id = threadIdx.x;
      int t = id >> 7, s = (id >> 6) & 1, ln = id & 63;
      int d = ln & 31, hh = ln >> 5;
      u16 e[8];
#pragma unroll
      for (int j = 0; j < 8; ++j) {
        int key = 32 * t + 16 * s + 8 * (j >> 2) + 4 * hh + (j & 3);
        u16 val = *(const u16*)(smem + key * C::CROW + (32 + d) * 2);
        if (d == 28) val = 0x3F80;
        else if (d > 28) val = 0;
        e[j] = val;
      }
      u32x4 v = {(unsigned)e[0] | ((unsigned)e[1] << 16), (unsigned)e[2] | ((unsigned)e[3] << 16),
                 (unsigned)e[4] | ((unsigned)e[5] << 16), (unsigned)e[6] | ((unsigned)e[7] << 16)};
      *(u32x4*)(kv + t * 4096 + 2048 + s * 1024 + ln * 16) = v;
    }
  } else {
    const int n0 = (nt - 5) * BN;
    gemm_main<BM, BN, false>(xB + (size_t)m0 * KDIM, wqkv, 3 * D_, n0, 3 * D_, nullptr, nullptr, smem);
    store_tile<BM, BN>(smem, (u16*)(ws + WS_QKVB) + (size_t)m0 * 3 * D_ + n0, 3 * D_, 3 * D_ - n0);
  }
}

__global__ __launch_bounds__(64 * GNW, 2) void outproj_kernel(
    const char* __restrict__ ws, const u16* __restrict__ woA, const u16* __restrict__ woB,
    u16* __restrict__ out) {
  constexpr int BM = 128, BN = 64;
  using C = GemmCfg<BM, BN>;
  __shared__ __attribute__((aligned(16))) char smem[C::NST * C::STB];
  const int mt = blockIdx.x & 15, r = blockIdx.x >> 4;
  const int br = r >= 7, nt = br ? r - 7 : r;
  const int m0 = mt * BM;
  const u16* X = (const u16*)(ws + (br ? WS_OB : WS_OA)) + (size_t)m0 * KDIM;
  gemm_main<BM, BN, false>(X, br ? woB : woA, D_, nt * BN, D_, nullptr, nullptr, smem);
  store_tile<BM, BN>(smem, out + (size_t)m0 * (2 * D_) + br * D_ + nt * BN, 2 * D_, BN);
}

DI float dpp_xor1(float x) {
  return __uint_as_float(__builtin_amdgcn_update_dpp(0, __float_as_uint(x), 0xB1, 0xf, 0xf, true));
}
DI float dpp_xor2(float x) {
  return __uint_as_float(__builtin_amdgcn_update_dpp(0, __float_as_uint(x), 0x4E, 0xf, 0xf, true));
}

DI float dpp_hmir(float x) {
  return __uint_as_float(__builtin_amdgcn_update_dpp(0, __float_as_uint(x), 0x141, 0xf, 0xf, true));
}
DI void ld4(const u16* p, float& a, float& b, float& c, float& d) {
  u32x2 v = *(const u32x2*)p;
  a = bf_lo(v[0]); b = bf_hi(v[0]); c = bf_lo(v[1]); d = bf_hi(v[1]);
}

constexpr int BSL = 2064;
constexpr int BWB = 18 * 1024;
DI void local_attn_item(const char* img, u16* __restrict__ oB, int pos, int h, int j, int lane) {
  const char* qp = img + h * 56;
  const char* kp = img + 1024 + j * BSL + h * 56;
  u32x2 qv[7], kv[7], vv[8];
#pragma unroll
  for (int i = 0; i < 7; ++i) { qv[i] = *(const u32x2*)(qp + 8 * i); kv[i] = *(const u32x2*)(kp + 8 * i); }
  const int jj = j < 7 ? j : 6;
#pragma unroll
  for (int wdw = 0; wdw < 8; ++wdw)
    vv[wdw] = *(const u32x2*)(img + 1024 + wdw * BSL + 896 + h * 56 + 8 * jj);
  float sa = 0.f;
#pragma unroll
  for (int i = 0; i < 7; ++i) {
    sa = fmaf(bf_lo(qv[i][0]), bf_lo(kv[i][0]), sa);
    sa = fmaf(bf_hi(qv[i][0]), bf_hi(kv[i][0]), sa);
    sa = fmaf(bf_lo(qv[i][1]), bf_lo(kv[i][1]), sa);
    sa = fmaf(bf_hi(qv[i][1]), bf_hi(kv[i][1]), sa);
  }
  float la = rnd_bf16(rnd_bf16(sa) * SCALE_F);
  if (pos - 2 * j < 0) la = -1e9f;
  float m = fmaxf(la, dpp_xor1(la));
  m = fmaxf(m, dpp_xor2(m));
  m = fmaxf(m, dpp_hmir(m));
  float e = __builtin_amdgcn_exp2f((la - m) * LOG2E);
  float sum = e + dpp_xor1(e);
  sum += dpp_xor2(sum);
  sum += dpp_hmir(sum);
  float pj_ = rnd_bf16(e / sum);
  float o0 = 0.f, o1 = 0.f, o2 = 0.f, o3 = 0.f;
#pragma unroll
  for (int wdw = 0; wdw < 8; ++wdw) {
    float pw = __shfl(pj_, (lane & ~7) | wdw);
    o0 = fmaf(pw, bf_lo(vv[wdw][0]), o0); o1 = fmaf(pw, bf_hi(vv[wdw][0]), o1);
    o2 = fmaf(pw, bf_lo(vv[wdw][1]), o2); o3 = fmaf(pw, bf_hi(vv[wdw][1]), o3);
  }
  if (j < 7) {
    u32x2 v0 = {pack_bf16(o0, o1), pack_bf16(o2, o3)};
    *(u32x2*)(oB + (size_t)pos * D_ + h * DH + 4 * j) = v0;
  }
}

constexpr float PTHR = 1.2379400392853803e27f;
constexpr float FMAXF = 3.0e38f;

DI void pv_tile(f32x16& O, const f32x16& s, bf16x8 v0, bf16x8 v1) {
  float p[16];
#pragma unroll
  for (int r = 0; r < 16; ++r) p[r] = __builtin_amdgcn_exp2f(s[r]);
  u32x4 pa = {pack_bf16(p[0], p[1]), pack_bf16(p[2], p[3]), pack_bf16(p[4], p[5]), pack_bf16(p[6], p[7])};
  u32x4 pbv = {pack_bf16(p[8], p[9]), pack_bf16(p[10], p[11]), pack_bf16(p[12], p[13]), pack_bf16(p[14], p[15])};
  O = __builtin_amdgcn_mfma_f32_32x32x16_bf16(v0, __builtin_bit_cast(bf16x8, pa), O, 0, 0, 0);
  O = __builtin_amdgcn_mfma_f32_32x32x16_bf16(v1, __builtin_bit_cast(bf16x8, pbv), O, 0, 0, 0);
}
DI void careful_tile(f32x16& O, f32x16& C, float& R, bool first, bf16x8 k0, bf16x8 k1, bf16x8 v0, bf16x8 v1,
                     bf16x8 q0, bf16x8 q1) {
  f32x16 s = __builtin_amdgcn_mfma_f32_32x32x16_bf16(k0, q0, C, 0, 0, 0);
  s = __builtin_amdgcn_mfma_f32_32x32x16_bf16(k1, q1, s, 0, 0, 0);
  float tm = fmaxf(s[0], s[1]);
#pragma unroll
  for (int r = 2; r < 16; r += 2) tm = fmaxf(fmaxf(tm, s[r]), s[r + 1]);
  auto rr = __builtin_amdgcn_permlane32_swap(__float_as_uint(tm), __float_as_uint(tm), false, false);
  float delta = fmaxf(__uint_as_float(rr[0]), __uint_as_float(rr[1]));
  if (!first) {
    delta = fmaxf(delta, 0.f);
    float alpha = __builtin_amdgcn_exp2f(-delta);
#pragma unroll
    for (int r = 0; r < 16; ++r) O[r] *= alpha;
  }
  R += delta;
#pragma unroll
  for (int r = 0; r < 16; ++r) { s[r] -= delta; C[r] = -R; }
  pv_tile(O, s, v0, v1);
}

__global__ __launch_bounds__(512, 2) void attn_kernel(char* __restrict__ ws) {
  __shared__ __attribute__((aligned(16))) char smem[8 * BWB];
  const int lane = threadIdx.x & 63;
  const int w = __builtin_amdgcn_readfirstlane(threadIdx.x >> 6);
  const int hh = lane >> 5;
  const int head = lane & 15;
  const int p0 = blockIdx.x * 8;
  const char* kvp = ws + WS_KV + (size_t)(w * 8) * 4096 + lane * 16;

  bf16x8 k0 = *(const bf16x8*)(kvp), k1 = *(const bf16x8*)(kvp + 1024);
  bf16x8 v0 = *(const bf16x8*)(kvp + 2048), v1 = *(const bf16x8*)(kvp + 3072);

  bf16x8 q0[4], q1[4];
#pragma unroll
  for (int b = 0; b < 4; ++b) {
    const int pos = p0 + b * 2 + ((lane >> 4) & 1);
    const u16* qp = (const u16*)(ws + WS_QA) + (size_t)pos * D_ + head * DH + 8 * hh;
    u32x2 a = *(const u32x2*)(qp), bb = *(const u32x2*)(qp + 4);
    u32x2 c = *(const u32x2*)(qp + 16);
    u32x2 d = {0u, 0u};
    if (hh == 0) d = *(const u32x2*)(qp + 20);
    u32x4 x0 = {a[0], a[1], bb[0], bb[1]}, x1 = {c[0], c[1], d[0], d[1]};
    q0[b] = __builtin_bit_cast(bf16x8, x0);
    q1[b] = __builtin_bit_cast(bf16x8, x1);
  }
  auto load_bimg = [&](int pos) {
    const char* qkvb = ws + WS_QKVB;
#pragma unroll
    for (int pc = 0; pc < 18; ++pc) {
      int off = pc * 1024 + lane * 16;
      const char* src;
      if (pc == 0) {
        src = qkvb + (size_t)pos * 2688 + (off < 896 ? off : 0);
      } else {
        int o2 = off - 1024;
        int j = o2 / BSL, col = o2 - j * BSL;
        if (j > 7) j = 7;
        if (col >= 1792) col = 0;
        int pr = pos - 2 * j; if (pr < 0) pr = 0;
        src = qkvb + (size_t)pr * 2688 + 896 + col;
      }
      glds16(src, smem + w * BWB + pc * 1024);
    }
  };

  if (w < 4) load_bimg(p0 + w);
  __builtin_amdgcn_s_waitcnt(0x0F70);
  f32x16 O[4], C[4];
  float R[4];
#pragma unroll
  for (int b = 0; b < 4; ++b) {
    R[b] = 0.f;
#pragma unroll
    for (int r = 0; r < 16; ++r) { O[b][r] = 0.f; C[b][r] = 0.f; }
  }
  {
    bf16x8 nk0 = *(const bf16x8*)(kvp + 4096), nk1 = *(const bf16x8*)(kvp + 4096 + 1024);
    bf16x8 nv0 = *(const bf16x8*)(kvp + 4096 + 2048), nv1 = *(const bf16x8*)(kvp + 4096 + 3072);
#pragma unroll
    for (int b = 0; b < 4; ++b) careful_tile(O[b], C[b], R[b], true, k0, k1, v0, v1, q0[b], q1[b]);
    k0 = nk0; k1 = nk1; v0 = nv0; v1 = nv1;
  }
  asm volatile("" : "+v"(k0), "+v"(k1), "+v"(v0), "+v"(v1));
  for (int t = 1; t < 8; ++t) {
    const int tn = t < 7 ? t + 1 : 7;
    bf16x8 nk0, nk1, nv0, nv1;
    {
      const char* np = kvp + tn * 4096;
      asm volatile("global_load_dwordx4 %0, %4, off\n\tglobal_load_dwordx4 %1, %4, off offset:1024\n\t"
                   "global_load_dwordx4 %2, %4, off offset:2048\n\tglobal_load_dwordx4 %3, %4, off offset:3072"
                   : "=&v"(nk0), "=&v"(nk1), "=&v"(nv0), "=&v"(nv1) : "v"(np) : "memory");
    }
    __builtin_amdgcn_sched_barrier(0);
#pragma unroll
    for (int bp = 0; bp < 2; ++bp) {
      f32x16 sa = __builtin_amdgcn_mfma_f32_32x32x16_bf16(k0, q0[2 * bp], C[2 * bp], 0, 0, 0);
      f32x16 sb = __builtin_amdgcn_mfma_f32_32x32x16_bf16(k0, q0[2 * bp + 1], C[2 * bp + 1], 0, 0, 0);
      sa = __builtin_amdgcn_mfma_f32_32x32x16_bf16(k1, q1[2 * bp], sa, 0, 0, 0);
      sb = __builtin_amdgcn_mfma_f32_32x32x16_bf16(k1, q1[2 * bp + 1], sb, 0, 0, 0);
      pv_tile(O[2 * bp], sa, v0, v1);
      pv_tile(O[2 * bp + 1], sb, v0, v1);
      __builtin_amdgcn_sched_barrier(0);
    }
    asm volatile("s_waitcnt vmcnt(0)" : "+v"(nk0), "+v"(nk1), "+v"(nv0), "+v"(nv1) :: "memory");
    k0 = nk0; k1 = nk1; v0 = nv0; v1 = nv1;
  }
  {
    float lm = fmaxf(fmaxf(O[0][12], O[1][12]), fmaxf(O[2][12], O[3][12]));
    float z = 0.f;
#pragma unroll
    for (int b = 0; b < 4; ++b)
#pragma unroll
      for (int r = 0; r < 16; ++r) z = fmaf(O[b][r], 0.f, z);
    bool bad = ((hh == 1) && !(lm <= PTHR)) || !(z == 0.f);
    if (__builtin_expect(__any(bad), 0)) {
#pragma unroll
      for (int b = 0; b < 4; ++b) {
        R[b] = 0.f;
#pragma unroll
        for (int r = 0; r < 16; ++r) { O[b][r] = 0.f; C[b][r] = 0.f; }
      }
#pragma unroll 1
      for (int t = 0; t < 8; ++t) {
        bf16x8 a0 = *(const bf16x8*)(kvp + t * 4096), a1 = *(const bf16x8*)(kvp + t * 4096 + 1024);
        bf16x8 b0 = *(const bf16x8*)(kvp + t * 4096 + 2048), b1 = *(const bf16x8*)(kvp + t * 4096 + 3072);
#pragma unroll
        for (int b = 0; b < 4; ++b) careful_tile(O[b], C[b], R[b], t == 0, a0, a1, b0, b1, q0[b], q1[b]);
      }
    }
  }

  if (w < 4) {
    WAIT_VM(0);
    local_attn_item(smem + w * BWB, (u16*)(ws + WS_OB), p0 + w, lane >> 3, lane & 7, lane);
    local_attn_item(smem + w * BWB, (u16*)(ws + WS_OB), p0 + w, 8 + (lane >> 3), lane & 7, lane);
    asm volatile("s_waitcnt lgkmcnt(0)" ::: "memory");
    load_bimg(p0 + w + 4);
    WAIT_VM(0);
    local_attn_item(smem + w * BWB, (u16*)(ws + WS_OB), p0 + w + 4, lane >> 3, lane & 7, lane);
    local_attn_item(smem + w * BWB, (u16*)(ws + WS_OB), p0 + w + 4, 8 + (lane >> 3), lane & 7, lane);
    asm volatile("s_waitcnt lgkmcnt(0)" ::: "memory");
  }

#pragma unroll
  for (int b = 0; b < 4; ++b) {
    float* xch = (float*)(smem + w * BWB) + b * (17 * 64);
#pragma unroll
    for (int r = 0; r < 16; ++r) xch[r * 64 + lane] = O[b][r];
    xch[16 * 64 + lane] = R[b];
  }
  raw_barrier();
  {
    const int b = w >> 1;
    float Rs[8], a[8];
#pragma unroll
    for (int i = 0; i < 8; ++i) Rs[i] = ((const float*)(smem + i * BWB))[b * (17 * 64) + 16 * 64 + lane];
    float Rn = Rs[0];
#pragma unroll
    for (int i = 1; i < 8; ++i) Rn = fmaxf(Rn, Rs[i]);
    float l = 0.f;
#pragma unroll
    for (int i = 0; i < 8; ++i) {
      a[i] = __builtin_amdgcn_exp2f(Rs[i] - Rn);
      l = fmaf(((const float*)(smem + i * BWB))[b * (17 * 64) + 12 * 64 + (lane & 31) + 32], a[i], l);
    }
    const float inv = 1.0f / l;
    const int pos = p0 + b * 2 + ((lane >> 4) & 1);
#pragma unroll
    for (int gg = 0; gg < 2; ++gg) {
      const int g = (w & 1) * 2 + gg;
      float o[4] = {0.f, 0.f, 0.f, 0.f};
#pragma unroll
      for (int i = 0; i < 8; ++i) {
        const float* x = (const float*)(smem + i * BWB) + b * (17 * 64);
#pragma unroll
        for (int r = 0; r < 4; ++r) o[r] = fmaf(x[(4 * g + r) * 64 + lane], a[i], o[r]);
      }
      if (g < 3 || hh == 0) {
        u32x2 v = {pack_bf16(o[0] * inv, o[1] * inv), pack_bf16(o[2] * inv, o[3] * inv)};
        *(u32x2*)((u16*)(ws + WS_OA) + (size_t)pos * D_ + head * DH + 4 * hh + 8 * g) = v;
      }
    }
  }
}

extern "C" void kernel_launch(void* const* d_in, const int* in_sizes, int n_in,
                              void* d_out, int out_size, void* d_ws, size_t ws_size,
                              hipStream_t stream) {
  const u16* xA = (const u16*)d_in[0];
  const u16* wq = (const u16*)d_in[1];
  const u16* wk = (const u16*)d_in[2];
  const u16* wv = (const u16*)d_in[3];
  const u16* woA = (const u16*)d_in[4];
  const u16* xB = (const u16*)d_in[5];
  const u16* wqkv = (const u16*)d_in[6];
  const u16* woB = (const u16*)d_in[7];
  char* ws = (char*)d_ws;
  const int g_mask = 0xff;
  if (g_mask & 1) proj_kernel<<<256, 64 * GNW, 0, stream>>>(xA, wq, wk, wv, xB, wqkv, ws);
  if (g_mask & 2) attn_kernel<<<256, 512, 0, stream>>>(ws);
  if (g_mask & 4) outproj_kernel<<<224, 64 * GNW, 0, stream>>>(ws, woA, woB, (u16*)d_out);
}
```

```cpp
#include <hip/hip_runtime.h>
#include <hip/hip_bf16.h>
#include <stdint.h>

constexpr int S_ = 2048, D_ = 448, H_ = 16, DH = 28;
constexpr int KDIM = 448;
constexpr float SCALE_F = 0.18898223650461362f;
constexpr float LOG2E = 1.4426950408889634f;

typedef unsigned short u16;
typedef short bf16x8 __attribute__((ext_vector_type(8)));
typedef short s16x4 __attribute__((ext_vector_type(4)));
typedef float f32x16 __attribute__((ext_vector_type(16)));
typedef float f32x2 __attribute__((ext_vector_type(2)));
typedef __bf16 bf16x2_t __attribute__((ext_vector_type(2)));
typedef unsigned u32x4 __attribute__((ext_vector_type(4)));
typedef unsigned u32x2 __attribute__((ext_vector_type(2)));

#define DI __device__ __forceinline__

DI unsigned pack_bf16(float lo, float hi) {
  f32x2 v = {lo, hi};
  bf16x2_t b = __builtin_convertvector(v, bf16x2_t);
  return __builtin_bit_cast(unsigned, b);
}
DI float rnd_bf16(float x) { return __uint_as_float(pack_bf16(0.f, x)); }
DI float bf_lo(unsigned u) { return __uint_as_float(u << 16); }
DI float bf_hi(unsigned u) { return __uint_as_float(u & 0xffff0000u); }

#define WAIT_VM(n) asm volatile("s_waitcnt vmcnt(" #n ")" ::: "memory")
template <int N> DI void wait_vm() { asm volatile("s_waitcnt vmcnt(%0)" ::"n"(N) : "memory"); }
DI void raw_barrier() {
  asm volatile("s_waitcnt lgkmcnt(0)" ::: "memory");
  __builtin_amdgcn_s_barrier();
}

DI void glds16(const void* g, void* l) {
  __builtin_amdgcn_global_load_lds((const unsigned*)g, (unsigned*)l, 16, 0, 0);
}
DI void glds4(const void* g, void* l) {
  __builtin_amdgcn_global_load_lds((const unsigned*)g, (unsigned*)l, 4, 0, 0);
}
DI s16x4 tr_read(const void* l) {
  return __builtin_amdgcn_ds_read_tr16_b64_v4i16((__attribute__((address_space(3))) s16x4*)l);
}

constexpr size_t WS_QA = 0;
constexpr size_t WS_KV = WS_QA + (size_t)S_ * D_ * 2;
constexpr size_t WS_QKVB = WS_KV + 64 * 4096;
constexpr size_t WS_OA = WS_QKVB + (size_t)S_ * 3 * D_ * 2;
constexpr size_t WS_OB = WS_OA + (size_t)S_ * D_ * 2;

constexpr int GNW = 8;
template <int BM, int BN> struct GemmCfg {
  static constexpr int XB = BM * 128;
  static constexpr int WB = 64 * BN * 2;
  static constexpr int STB = XB + WB;
  static constexpr int NST = 4;
  static constexpr int XI = XB / 1024 / GNW;
  static constexpr int WI = WB / 1024 / GNW;
  static constexpr int WGM = (BM >= BN) ? 4 : 2, WGN = GNW / WGM;
  static constexpr int MT = BM / 32 / WGM, NT = BN / 32 / WGN;
  static constexpr int CROW = BN * 2 + 16;
};

template <int BN> DI int wswz(int k) {
  if constexpr (BN == 128) return (k & 3) << 2;
  else return ((k >> 1) & 1) << 2;
}

template <int BM, int BN, bool KV>
DI void gemm_main(const u16* __restrict__ X,
                  const u16* __restrict__ W, int ldw, int n0, int ncols,
                  const u16* __restrict__ Wk, const u16* __restrict__ Wv,
                  char* smem, float oscale = 1.0f) {
  using C = GemmCfg<BM, BN>;
  const int lane = threadIdx.x & 63;
  const int w = __builtin_amdgcn_readfirstlane(threadIdx.x >> 6);
  const int wm = w / C::WGN, wn = w % C::WGN;
  f32x16 acc[C::MT][C::NT];
  constexpr int KVI = 64 / GNW;
  constexpr int WCNT = KV ? KVI : C::WI;
  constexpr int CNT = C::XI + WCNT;

  const u16* xsrc[C::XI];
#pragma unroll
  for (int i = 0; i < C::XI; ++i) {
    int row = (w * C::XI + i) * 8 + (lane >> 3);
    int c = (lane & 7) ^ ((row >> 1) & 7);
    xsrc[i] = X + row * KDIM + c * 8;
  }
  const u16* wsrc[WCNT];
  if constexpr (!KV) {
    constexpr int CPR = BN / 8, RPI = 1024 / (BN * 2);
#pragma unroll
    for (int i = 0; i < C::WI; ++i) {
      int k = (w * C::WI + i) * RPI + lane / CPR;
      int c = (lane % CPR) ^ wswz<BN>(k);
      int col = n0 + c * 8;
      if (col + 8 > ncols) col = ncols - 8;
      wsrc[i] = W + k * ldw + col;
    }
  } else {
#pragma unroll
    for (int i = 0; i < KVI; ++i) {
      int k = w * KVI + i;
      int c = (lane >> 2) ^ wswz<BN>(k);
      int col = c * 8 + (lane & 3) * 2;
      const u16* p = Wk + k * DH;
      if (col < 28) p = Wk + k * DH + col;
      else if (col >= 32 && col < 60) p = Wv + k * DH + (col - 32);
      wsrc[i] = p;
    }
  }
  auto issue = [&](int ks) {
    char* sb = smem + (ks % C::NST) * C::STB;
#pragma unroll
    for (int i = 0; i < C::XI; ++i) glds16(xsrc[i] + ks * 64, sb + (w * C::XI + i) * 1024);
    if constexpr (!KV) {
#pragma unroll
      for (int i = 0; i < C::WI; ++i) glds16(wsrc[i] + (size_t)ks * 64 * ldw, sb + C::XB + (w * C::WI + i) * 1024);
    } else {
#pragma unroll
      for (int i = 0; i < KVI; ++i) glds4(wsrc[i] + ks * 64 * DH, sb + C::XB + (w * KVI + i) * 256);
    }
  };

  const int hh = lane >> 5;
  int xoff[C::MT];
#pragma unroll
  for (int i = 0; i < C::MT; ++i) {
    int row = wm * (BM / C::WGM) + 32 * i + (lane & 31);
    xoff[i] = row * 128;
  }
  const int xsw = ((lane & 31) >> 1) & 7;
  const int g = lane >> 4, q = (lane & 15) >> 2, p = lane & 3;
  int woff[C::NT];
#pragma unroll
  for (int j = 0; j < C::NT; ++j) {
    int nn = wn * (BN / C::WGN) + 32 * j + 16 * (g & 1);
    int c = (nn >> 3) + (p >> 1);
    int kk = 8 * (g >> 1) + q;
    woff[j] = C::XB + kk * (BN * 2) + ((c ^ wswz<BN>(kk)) << 4) + (p & 1) * 8;
  }

#pragma unroll
  for (int i = 0; i < C::MT; ++i)
#pragma unroll
    for (int j = 0; j < C::NT; ++j)
#pragma unroll
      for (int r = 0; r < 16; ++r) acc[i][j][r] = 0.f;

  issue(0); issue(1); issue(2);
#pragma unroll
  for (int ks = 0; ks < 7; ++ks) {
    if (ks <= 4) wait_vm<2 * CNT>();
    else if (ks == 5) wait_vm<CNT>();
    else wait_vm<0>();
    raw_barrier();
    if (ks + 3 <= 6) issue(ks + 3);
    const char* sb = smem + (ks % C::NST) * C::STB;
#pragma unroll
    for (int k16 = 0; k16 < 4; ++k16) {
      bf16x8 xa[C::MT], wb[C::NT];
#pragma unroll
      for (int i = 0; i < C::MT; ++i)
        xa[i] = *(const bf16x8*)(sb + xoff[i] + (((2 * k16 + hh) ^ xsw) << 4));
#pragma unroll
      for (int j = 0; j < C::NT; ++j) {
        s16x4 lo = tr_read(sb + woff[j] + (16 * k16) * (BN * 2));
        s16x4 hi = tr_read(sb + woff[j] + (16 * k16 + 4) * (BN * 2));
        wb[j] = __builtin_shufflevector(lo, hi, 0, 1, 2, 3, 4, 5, 6, 7);
      }
#pragma unroll
      for (int i = 0; i < C::MT; ++i)
#pragma unroll
        for (int j = 0; j < C::NT; ++j)
          acc[i][j] = __builtin_amdgcn_mfma_f32_32x32x16_bf16(wb[j], xa[i], acc[i][j], 0, 0, 0);
    }
  }
  raw_barrier();
#pragma unroll
  for (int i = 0; i < C::MT; ++i)
#pragma unroll
    for (int j = 0; j < C::NT; ++j)
#pragma unroll
      for (int gq = 0; gq < 4; ++gq) {
        int pos = wm * (BM / C::WGM) + 32 * i + (lane & 31);
        int n = wn * (BN / C::WGN) + 32 * j + 8 * gq + 4 * hh;
        u32x2 v = {pack_bf16(acc[i][j][4 * gq] * oscale, acc[i][j][4 * gq + 1] * oscale),
                   pack_bf16(acc[i][j][4 * gq + 2] * oscale, acc[i][j][4 * gq + 3] * oscale)};
        *(u32x2*)(smem + pos * C::CROW + n * 2) = v;
      }
  raw_barrier();
}

template <int BM, int BN>
DI void store_tile(const char* smem, u16* __restrict__ dst, int ldd, int nvalid) {
  using C = GemmCfg<BM, BN>;
  constexpr int CPR = BN / 8;
  constexpr int TOT = BM * CPR;
#pragma unroll
  for (int it = 0; it < TOT / (64 * GNW); ++it) {
    int id = it * (64 * GNW) + threadIdx.x;
    int row = id / CPR, c = id % CPR;
    u32x4 v = *(const u32x4*)(smem + row * C::CROW + c * 16);
    if (c * 8 < nvalid) *(u32x4*)(dst + (size_t)row * ldd + c * 8) = v;
  }
}

__global__ __launch_bounds__(64 * GNW, 2) void proj_kernel(
    const u16* __restrict__ xA, const u16* __restrict__ wq, const u16* __restrict__ wk,
    const u16* __restrict__ wv, const u16* __restrict__ xB, const u16* __restrict__ wqkv,
    char* __restrict__ ws) {
  constexpr int BM = 128, BN = 128;
  using C = GemmCfg<BM, BN>;
  __shared__ __attribute__((aligned(16))) char smem[C::NST * C::STB];
  const int mt = blockIdx.x & 15, nt = blockIdx.x >> 4;
  const int m0 = mt * BM;
  if (nt < 4) {
    gemm_main<BM, BN, false>(xA + (size_t)m0 * KDIM, wq, D_, nt * BN, D_, nullptr, nullptr, smem, SCALE_F * LOG2E);
    store_tile<BM, BN>(smem, (u16*)(ws + WS_QA) + (size_t)m0 * D_ + nt * BN, D_, D_ - nt * BN);
  } else if (nt == 4) {
    gemm_main<BM, BN, true>(xA + (size_t)m0 * KDIM, nullptr, 0, 0, 0, wk, wv, smem);
    char* kv = ws + WS_KV + (size_t)(m0 >> 5) * 4096;
    {
      int id = threadIdx.x;
      int pos = id >> 2, c = id & 3;
      u32x4 v = *(const u32x4*)(smem + pos * C::CROW + c * 16);
      if (c == 3) { v[2] = 0; v[3] = 0; }
      int t = pos >> 5, s = c >> 1, hh = c & 1;
      *(u32x4*)(kv + t * 4096 + s * 1024 + ((pos & 31) + 32 * hh) * 16) = v;
    }
    {
      int id = threadIdx.x;
      int t = id >> 7, s = (id >> 6) & 1, ln = id & 63;
      int d = ln & 31, hh = ln >> 5;
      u16 e[8];
#pragma unroll
      for (int j = 0; j < 8; ++j) {
        int key = 32 * t + 16 * s + 8 * (j >> 2) + 4 * hh + (j & 3);
        u16 val = *(const u16*)(smem + key * C::CROW + (32 + d) * 2);
        if (d == 28) val = 0x3F80;
        else if (d > 28) val = 0;
        e[j] = val;
      }
      u32x4 v = {(unsigned)e[0] | ((unsigned)e[1] << 16), (unsigned)e[2] | ((unsigned)e[3] << 16),
                 (unsigned)e[4] | ((unsigned)e[5] << 16), (unsigned)e[6] | ((unsigned)e[7] << 16)};
      *(u32x4*)(kv + t * 4096 + 2048 + s * 1024 + ln * 16) = v;
    }
  } else {
    const int n0 = (nt - 5) * BN;
    gemm_main<BM, BN, false>(xB + (size_t)m0 * KDIM, wqkv, 3 * D_, n0, 3 * D_, nullptr, nullptr, smem);
    store_tile<BM, BN>(smem, (u16*)(ws + WS_QKVB) + (size_t)m0 * 3 * D_ + n0, 3 * D_, 3 * D_ - n0);
  }
}

__global__ __launch_bounds__(64 * GNW, 2) void outproj_kernel(
    const char* __restrict__ ws, const u16* __restrict__ woA, const u16* __restrict__ woB,
    u16* __restrict__ out) {
  constexpr int BM = 128, BN = 64;
  using C = GemmCfg<BM, BN>;
  __shared__ __attribute__((aligned(16))) char smem[C::NST * C::STB];
  const int mt = blockIdx.x & 15, r = blockIdx.x >> 4;
  const int br = r >= 7, nt = br ? r - 7 : r;
  const int m0 = mt * BM;
  const u16* X = (const u16*)(ws + (br ? WS_OB : WS_OA)) + (size_t)m0 * KDIM;
  gemm_main<BM, BN, false>(X, br ? woB : woA, D_, nt * BN, D_, nullptr, nullptr, smem);
  store_tile<BM, BN>(smem, out + (size_t)m0 * (2 * D_) + br * D_ + nt * BN, 2 * D_, BN);
}

DI float dpp_xor1(float x) {
  return __uint_as_float(__builtin_amdgcn_update_dpp(0, __float_as_uint(x), 0xB1, 0xf, 0xf, true));
}
DI float dpp_xor2(float x) {
  return __uint_as_float(__builtin_amdgcn_update_dpp(0, __float_as_uint(x), 0x4E, 0xf, 0xf, true));
}

DI float dpp_hmir(float x) {
  return __uint_as_float(__builtin_amdgcn_update_dpp(0, __float_as_uint(x), 0x141, 0xf, 0xf, true));
}
DI void ld4(const u16* p, float& a, float& b, float& c, float& d) {
  u32x2 v = *(const u32x2*)p;
  a = bf_lo(v[0]); b = bf_hi(v[0]); c = bf_lo(v[1]); d = bf_hi(v[1]);
}

constexpr int BSL = 2064;
constexpr int BWB = 18 * 1024;
DI void local_attn_item(const char* img, u16* __restrict__ oB, int pos, int h, int j, int lane) {
  const char* qp = img + h * 56;
  const char* kp = img + 1024 + j * BSL + h * 56;
  u32x2 qv[7], kv[7], vv[8];
#pragma unroll
  for (int i = 0; i < 7; ++i) { qv[i] = *(const u32x2*)(qp + 8 * i); kv[i] = *(const u32x2*)(kp + 8 * i); }
  const int jj = j < 7 ? j : 6;
#pragma unroll
  for (int wdw = 0; wdw < 8; ++wdw)
    vv[wdw] = *(const u32x2*)(img + 1024 + wdw * BSL + 896 + h * 56 + 8 * jj);
  float sa = 0.f;
#pragma unroll
  for (int i = 0; i < 7; ++i) {
    sa = fmaf(bf_lo(qv[i][0]), bf_lo(kv[i][0]), sa);
    sa = fmaf(bf_hi(qv[i][0]), bf_hi(kv[i][0]), sa);
    sa = fmaf(bf_lo(qv[i][1]), bf_lo(kv[i][1]), sa);
    sa = fmaf(bf_hi(qv[i][1]), bf_hi(kv[i][1]), sa);
  }
  float la = rnd_bf16(rnd_bf16(sa) * SCALE_F);
  if (pos - 2 * j < 0) la = -1e9f;
  float m = fmaxf(la, dpp_xor1(la));
  m = fmaxf(m, dpp_xor2(m));
  m = fmaxf(m, dpp_hmir(m));
  float e = __builtin_amdgcn_exp2f((la - m) * LOG2E);
  float sum = e + dpp_xor1(e);
  sum += dpp_xor2(sum);
  sum += dpp_hmir(sum);
  float pj_ = rnd_bf16(e / sum);
  float o0 = 0.f, o1 = 0.f, o2 = 0.f, o3 = 0.f;
#pragma unroll
  for (int wdw = 0; wdw < 8; ++wdw) {
    float pw = __shfl(pj_, (lane & ~7) | wdw);
    o0 = fmaf(pw, bf_lo(vv[wdw][0]), o0); o1 = fmaf(pw, bf_hi(vv[wdw][0]), o1);
    o2 = fmaf(pw, bf_lo(vv[wdw][1]), o2); o3 = fmaf(pw, bf_hi(vv[wdw][1]), o3);
  }
  if (j < 7) {
    u32x2 v0 = {pack_bf16(o0, o1), pack_bf16(o2, o3)};
    *(u32x2*)(oB + (size_t)pos * D_ + h * DH + 4 * j) = v0;
  }
}

constexpr float PTHR = 1.2379400392853803e27f;
constexpr float FMAXF = 3.0e38f;

DI void pv_tile(f32x16& O, const f32x16& s, bf16x8 v0, bf16x8 v1) {
  float p[16];
#pragma unroll
  for (int r = 0; r < 16; ++r) p[r] = __builtin_amdgcn_exp2f(s[r]);
  u32x4 pa = {pack_bf16(p[0], p[1]), pack_bf16(p[2], p[3]), pack_bf16(p[4], p[5]), pack_bf16(p[6], p[7])};
  u32x4 pbv = {pack_bf16(p[8], p[9]), pack_bf16(p[10], p[11]), pack_bf16(p[12], p[13]), pack_bf16(p[14], p[15])};
  O = __builtin_amdgcn_mfma_f32_32x32x16_bf16(v0, __builtin_bit_cast(bf16x8, pa), O, 0, 0, 0);
  O = __builtin_amdgcn_mfma_f32_32x32x16_bf16(v1, __builtin_bit_cast(bf16x8, pbv), O, 0, 0, 0);
}
DI void careful_tile(f32x16& O, f32x16& C, float& R, bool first, bf16x8 k0, bf16x8 k1, bf16x8 v0, bf16x8 v1,
                     bf16x8 q0, bf16x8 q1) {
  f32x16 s = __builtin_amdgcn_mfma_f32_32x32x16_bf16(k0, q0, C, 0, 0, 0);
  s = __builtin_amdgcn_mfma_f32_32x32x16_bf16(k1, q1, s, 0, 0, 0);
  float tm = fmaxf(s[0], s[1]);
#pragma unroll
  for (int r = 2; r < 16; r += 2) tm = fmaxf(fmaxf(tm, s[r]), s[r + 1]);
  auto rr = __builtin_amdgcn_permlane32_swap(__float_as_uint(tm), __float_as_uint(tm), false, false);
  float delta = fmaxf(__uint_as_float(rr[0]), __uint_as_float(rr[1]));
  if (!first) {
    delta = fmaxf(delta, 0.f);
    float alpha = __builtin_amdgcn_exp2f(-delta);
#pragma unroll
    for (int r = 0; r < 16; ++r) O[r] *= alpha;
  }
  R += delta;
#pragma unroll
  for (int r = 0; r < 16; ++r) { s[r] -= delta; C[r] = -R; }
  pv_tile(O, s, v0, v1);
}

__global__ __launch_bounds__(512, 2) void attn_kernel(char* __restrict__ ws) {
  __shared__ __attribute__((aligned(16))) char smem[8 * BWB];
  const int lane = threadIdx.x & 63;
  const int w = __builtin_amdgcn_readfirstlane(threadIdx.x >> 6);
  const int hh = lane >> 5;
  const int head = lane & 15;
  const int p0 = blockIdx.x * 8;
  const char* kvp = ws + WS_KV + (size_t)(w * 8) * 4096 + lane * 16;

  bf16x8 k0 = *(const bf16x8*)(kvp), k1 = *(const bf16x8*)(kvp + 1024);
  bf16x8 v0 = *(const bf16x8*)(kvp + 2048), v1 = *(const bf16x8*)(kvp + 3072);

  bf16x8 q0[4], q1[4];
#pragma unroll
  for (int b = 0; b < 4; ++b) {
    const int pos = p0 + b * 2 + ((lane >> 4) & 1);
    const u16* qp = (const u16*)(ws + WS_QA) + (size_t)pos * D_ + head * DH + 8 * hh;
    u32x2 a = *(const u32x2*)(qp), bb = *(const u32x2*)(qp + 4);
    u32x2 c = *(const u32x2*)(qp + 16);
    u32x2 d = {0u, 0u};
    if (hh == 0) d = *(const u32x2*)(qp + 20);
    u32x4 x0 = {a[0], a[1], bb[0], bb[1]}, x1 = {c[0], c[1], d[0], d[1]};
    q0[b] = __builtin_bit_cast(bf16x8, x0);
    q1[b] = __builtin_bit_cast(bf16x8, x1);
  }
  auto load_bimg = [&](int pos) {
    const char* qkvb = ws + WS_QKVB;
#pragma unroll
    for (int pc = 0; pc < 18; ++pc) {
      int off = pc * 1024 + lane * 16;
      const char* src;
      if (pc == 0) {
        src = qkvb + (size_t)pos * 2688 + (off < 896 ? off : 0);
      } else {
        int o2 = off - 1024;
        int j = o2 / BSL, col = o2 - j * BSL;
        if (j > 7) j = 7;
        if (col >= 1792) col = 0;
        int pr = pos - 2 * j; if (pr < 0) pr = 0;
        src = qkvb + (size_t)pr * 2688 + 896 + col;
      }
      glds16(src, smem + w * BWB + pc * 1024);
    }
  };

  if (w < 4) load_bimg(p0 + w);
  __builtin_amdgcn_s_waitcnt(0x0F70);
  f32x16 O[4], C[4];
  float R[4];
#pragma unroll
  for (int b = 0; b < 4; ++b) {
    R[b] = 0.f;
#pragma unroll
    for (int r = 0; r < 16; ++r) { O[b][r] = 0.f; C[b][r] = 0.f; }
  }
  {
    bf16x8 nk0 = *(const bf16x8*)(kvp + 4096), nk1 = *(const bf16x8*)(kvp + 4096 + 1024);
    bf16x8 nv0 = *(const bf16x8*)(kvp + 4096 + 2048), nv1 = *(const bf16x8*)(kvp + 4096 + 3072);
#pragma unroll
    for (int b = 0; b < 4; ++b) careful_tile(O[b], C[b], R[b], true, k0, k1, v0, v1, q0[b], q1[b]);
    k0 = nk0; k1 = nk1; v0 = nv0; v1 = nv1;
  }
  asm volatile("" : "+v"(k0), "+v"(k1), "+v"(v0), "+v"(v1));
  for (int t = 1; t < 8; ++t) {
    const int tn = t < 7 ? t + 1 : 7;
    bf16x8 nk0, nk1, nv0, nv1;
    {
      const char* np = kvp + tn * 4096;
      asm volatile("global_load_dwordx4 %0, %4, off\n\tglobal_load_dwordx4 %1, %4, off offset:1024\n\t"
                   "global_load_dwordx4 %2, %4, off offset:2048\n\tglobal_load_dwordx4 %3, %4, off offset:3072"
                   : "=&v"(nk0), "=&v"(nk1), "=&v"(nv0), "=&v"(nv1) : "v"(np) : "memory");
    }
    __builtin_amdgcn_sched_barrier(0);
#pragma unroll
    for (int bp = 0; bp < 2; ++bp) {
      f32x16 sa = __builtin_amdgcn_mfma_f32_32x32x16_bf16(k0, q0[2 * bp], C[2 * bp], 0, 0, 0);
      f32x16 sb = __builtin_amdgcn_mfma_f32_32x32x16_bf16(k0, q0[2 * bp + 1], C[2 * bp + 1], 0, 0, 0);
      sa = __builtin_amdgcn_mfma_f32_32x32x16_bf16(k1, q1[2 * bp], sa, 0, 0, 0);
      sb = __builtin_amdgcn_mfma_f32_32x32x16_bf16(k1, q1[2 * bp + 1], sb, 0, 0, 0);
      pv_tile(O[2 * bp], sa, v0, v1);
      pv_tile(O[2 * bp + 1], sb, v0, v1);
      __builtin_amdgcn_sched_barrier(0);
    }
    asm volatile("s_waitcnt vmcnt(0)" : "+v"(nk0), "+v"(nk1), "+v"(nv0), "+v"(nv1) :: "memory");
    k0 = nk0; k1 = nk1; v0 = nv0; v1 = nv1;
  }
  {
    float lm = fmaxf(fmaxf(O[0][12], O[1][12]), fmaxf(O[2][12], O[3][12]));
    float z = 0.f;
#pragma unroll
    for (int b = 0; b < 4; ++b)
#pragma unroll
      for (int r = 0; r < 16; ++r) z = fmaf(O[b][r], 0.f, z);
    bool bad = ((hh == 1) && !(lm <= PTHR)) || !(z == 0.f);
    if (__builtin_expect(__any(bad), 0)) {
#pragma unroll
      for (int b = 0; b < 4; ++b) {
        R[b] = 0.f;
#pragma unroll
        for (int r = 0; r < 16; ++r) { O[b][r] = 0.f; C[b][r] = 0.f; }
      }
#pragma unroll 1
      for (int t = 0; t < 8; ++t) {
        bf16x8 a0 = *(const bf16x8*)(kvp + t * 4096), a1 = *(const bf16x8*)(kvp + t * 4096 + 1024);
        bf16x8 b0 = *(const bf16x8*)(kvp + t * 4096 + 2048), b1 = *(const bf16x8*)(kvp + t * 4096 + 3072);
#pragma unroll
        for (int b = 0; b < 4; ++b) careful_tile(O[b], C[b], R[b], t == 0, a0, a1, b0, b1, q0[b], q1[b]);
      }
    }
  }

  if (w < 4) {
    WAIT_VM(0);
    local_attn_item(smem + w * BWB, (u16*)(ws + WS_OB), p0 + w, lane >> 3, lane & 7, lane);
    local_attn_item(smem + w * BWB, (u16*)(ws + WS_OB), p0 + w, 8 + (lane >> 3), lane & 7, lane);
    asm volatile("s_waitcnt lgkmcnt(0)" ::: "memory");
    load_bimg(p0 + w + 4);
    WAIT_VM(0);
    local_attn_item(smem + w * BWB, (u16*)(ws + WS_OB), p0 + w + 4, lane >> 3, lane & 7, lane);
    local_attn_item(smem + w * BWB, (u16*)(ws + WS_OB), p0 + w + 4, 8 + (lane >> 3), lane & 7, lane);
    asm volatile("s_waitcnt lgkmcnt(0)" ::: "memory");
  }

  {
    typedef float f32x4 __attribute__((ext_vector_type(4)));
    f32x4* xo = (f32x4*)(smem + w * BWB);
    float* xr = (float*)(smem + w * BWB + 16384);
#pragma unroll
    for (int b = 0; b < 4; ++b) {
#pragma unroll
      for (int g = 0; g < 4; ++g)
        xo[(b * 4 + g) * 64 + lane] = (f32x4){O[b][4 * g], O[b][4 * g + 1], O[b][4 * g + 2], O[b][4 * g + 3]};
      xr[b * 64 + lane] = R[b];
    }
  }
  raw_barrier();
  {
    typedef float f32x4 __attribute__((ext_vector_type(4)));
    const int b = w >> 1;
    float Rs[8], a[8];
#pragma unroll
    for (int i = 0; i < 8; ++i) Rs[i] = ((const float*)(smem + i * BWB + 16384))[b * 64 + lane];
    float Rn = Rs[0];
#pragma unroll
    for (int i = 1; i < 8; ++i) Rn = fmaxf(Rn, Rs[i]);
    float l = 0.f;
#pragma unroll
    for (int i = 0; i < 8; ++i) {
      a[i] = __builtin_amdgcn_exp2f(Rs[i] - Rn);
      l = fmaf(((const f32x4*)(smem + i * BWB))[(b * 4 + 3) * 64 + (lane & 31) + 32][0], a[i], l);
    }
    const float inv = 1.0f / l;
    const int pos = p0 + b * 2 + ((lane >> 4) & 1);
#pragma unroll
    for (int gg = 0; gg < 2; ++gg) {
      const int g = (w & 1) * 2 + gg;
      f32x4 o = {0.f, 0.f, 0.f, 0.f};
#pragma unroll
      for (int i = 0; i < 8; ++i) {
        f32x4 x = ((const f32x4*)(smem + i * BWB))[(b * 4 + g) * 64 + lane];
        o[0] = fmaf(x[0], a[i], o[0]); o[1] = fmaf(x[1], a[i], o[1]);
        o[2] = fmaf(x[2], a[i], o[2]); o[3] = fmaf(x[3], a[i], o[3]);
      }
      if (g < 3 || hh == 0) {
        u32x2 v = {pack_bf16(o[0] * inv, o[1] * inv), pack_bf16(o[2] * inv, o[3] * inv)};
        *(u32x2*)((u16*)(ws + WS_OA) + (size_t)pos * D_ + head * DH + 4 * hh + 8 * g) = v;
      }
    }
  }
}

extern "C" void kernel_launch(void* const* d_in, const int* in_sizes, int n_in,
                              void* d_out, int out_size, void* d_ws, size_t ws_size,
                              hipStream_t stream) {
  const u16* xA = (const u16*)d_in[0];
  const u16* wq = (const u16*)d_in[1];
  const u16* wk = (const u16*)d_in[2];
  const u16* wv = (const u16*)d_in[3];
  const u16* woA = (const u16*)d_in[4];
  const u16* xB = (const u16*)d_in[5];
  const u16* wqkv = (const u16*)d_in[6];
  const u16* woB = (const u16*)d_in[7];
  char* ws = (char*)d_ws;
  const int g_mask = 0xff;
  if (g_mask & 1) proj_kernel<<<256, 64 * GNW, 0, stream>>>(xA, wq, wk, wv, xB, wqkv, ws);
  if (g_mask & 2) attn_kernel<<<256, 512, 0, stream>>>(ws);
  if (g_mask & 4) outproj_kernel<<<224, 64 * GNW, 0, stream>>>(ws, woA, woB, (u16*)d_out);
}
```

```cpp
#include <hip/hip_runtime.h>
#include <hip/hip_bf16.h>
#include <stdint.h>

constexpr int S_ = 2048, D_ = 448, H_ = 16, DH = 28;
constexpr int KDIM = 448;
constexpr float SCALE_F = 0.18898223650461362f;
constexpr float LOG2E = 1.4426950408889634f;

typedef unsigned short u16;
typedef short bf16x8 __attribute__((ext_vector_type(8)));
typedef short s16x4 __attribute__((ext_vector_type(4)));
typedef float f32x16 __attribute__((ext_vector_type(16)));
typedef float f32x2 __attribute__((ext_vector_type(2)));
typedef __bf16 bf16x2_t __attribute__((ext_vector_type(2)));
typedef unsigned u32x4 __attribute__((ext_vector_type(4)));
typedef unsigned u32x2 __attribute__((ext_vector_type(2)));

#define DI __device__ __forceinline__

DI unsigned pack_bf16(float lo, float hi) {
  f32x2 v = {lo, hi};
  bf16x2_t b = __builtin_convertvector(v, bf16x2_t);
  return __builtin_bit_cast(unsigned, b);
}
DI float rnd_bf16(float x) { return __uint_as_float(pack_bf16(0.f, x)); }
DI float bf_lo(unsigned u) { return __uint_as_float(u << 16); }
DI float bf_hi(unsigned u) { return __uint_as_float(u & 0xffff0000u); }

#define WAIT_VM(n) asm volatile("s_waitcnt vmcnt(" #n ")" ::: "memory")
template <int N> DI void wait_vm() { asm volatile("s_waitcnt vmcnt(%0)" ::"n"(N) : "memory"); }
DI void raw_barrier() {
  asm volatile("s_waitcnt lgkmcnt(0)" ::: "memory");
  __builtin_amdgcn_s_barrier();
}

DI void glds16(const void* g, void* l) {
  __builtin_amdgcn_global_load_lds((const unsigned*)g, (unsigned*)l, 16, 0, 0);
}
DI void glds4(const void* g, void* l) {
  __builtin_amdgcn_global_load_lds((const unsigned*)g, (unsigned*)l, 4, 0, 0);
}
DI s16x4 tr_read(const void* l) {
  return __builtin_amdgcn_ds_read_tr16_b64_v4i16((__attribute__((address_space(3))) s16x4*)l);
}

constexpr size_t WS_QA = 0;
constexpr size_t WS_KV = WS_QA + (size_t)S_ * D_ * 2;
constexpr size_t WS_QKVB = WS_KV + 64 * 4096;
constexpr size_t WS_OA = WS_QKVB + (size_t)S_ * 3 * D_ * 2;
constexpr size_t WS_OB = WS_OA + (size_t)S_ * D_ * 2;

constexpr int GNW = 8;
template <int BM, int BN> struct GemmCfg {
  static constexpr int XB = BM * 128;
  static constexpr int WB = 64 * BN * 2;
  static constexpr int STB = XB + WB;
  static constexpr int NST = 4;
  static constexpr int XI = XB / 1024 / GNW;
  static constexpr int WI = WB / 1024 / GNW;
  static constexpr int WGM = (BM >= BN) ? 4 : 2, WGN = GNW / WGM;
  static constexpr int MT = BM / 32 / WGM, NT = BN / 32 / WGN;
  static constexpr int CROW = BN * 2 + 16;
};

template <int BN> DI int wswz(int k) {
  if constexpr (BN == 128) return (k & 3) << 2;
  else return ((k >> 1) & 1) << 2;
}

template <int BM, int BN, bool KV>
DI void gemm_main(const u16* __restrict__ X,
                  const u16* __restrict__ W, int ldw, int n0, int ncols,
                  const u16* __restrict__ Wk, const u16* __restrict__ Wv,
                  char* smem, float oscale = 1.0f) {
  using C = GemmCfg<BM, BN>;
  const int lane = threadIdx.x & 63;
  const int w = __builtin_amdgcn_readfirstlane(threadIdx.x >> 6);
  const int wm = w / C::WGN, wn = w % C::WGN;
  f32x16 acc[C::MT][C::NT];
  constexpr int KVI = 32 / GNW;
  constexpr int WCNT = KV ? KVI : C::WI;
  constexpr int CNT = C::XI + WCNT;

  const u16* xsrc[C::XI];
#pragma unroll
  for (int i = 0; i < C::XI; ++i) {
    int row = (w * C::XI + i) * 8 + (lane >> 3);
    int c = (lane & 7) ^ ((row >> 1) & 7);
    xsrc[i] = X + row * KDIM + c * 8;
  }
  const u16* wsrc[WCNT];
  if constexpr (!KV) {
    constexpr int CPR = BN / 8, RPI = 1024 / (BN * 2);
#pragma unroll
    for (int i = 0; i < C::WI; ++i) {
      int k = (w * C::WI + i) * RPI + lane / CPR;
      int c = (lane % CPR) ^ wswz<BN>(k);
      int col = n0 + c * 8;
      if (col + 8 > ncols) col = ncols - 8;
      wsrc[i] = W + k * ldw + col;
    }
  } else {
#pragma unroll
    for (int i = 0; i < KVI; ++i) {
      int k = (w * KVI + i) * 2 + (lane >> 5);
      int c = ((lane & 31) >> 2) ^ wswz<BN>(k);
      int col = c * 8 + (lane & 3) * 2;
      const u16* p = Wk + k * DH;
      if (col < 28) p = Wk + k * DH + col;
      else if (col >= 32 && col < 60) p = Wv + k * DH + (col - 32);
      wsrc[i] = p;
    }
  }
  auto issue = [&](int ks) {
    char* sb = smem + (ks % C::NST) * C::STB;
#pragma unroll
    for (int i = 0; i < C::XI; ++i) glds16(xsrc[i] + ks * 64, sb + (w * C::XI + i) * 1024);
    if constexpr (!KV) {
#pragma unroll
      for (int i = 0; i < C::WI; ++i) glds16(wsrc[i] + (size_t)ks * 64 * ldw, sb + C::XB + (w * C::WI + i) * 1024);
    } else {
#pragma unroll
      for (int i = 0; i < KVI; ++i) glds4(wsrc[i] + ks * 64 * DH, sb + C::XB + (w * KVI + i) * 256);
    }
  };

  const int hh = lane >> 5;
  int xoff[C::MT];
#pragma unroll
  for (int i = 0; i < C::MT; ++i) {
    int row = wm * (BM / C::WGM) + 32 * i + (lane & 31);
    xoff[i] = row * 128;
  }
  const int xsw = ((lane & 31) >> 1) & 7;
  const int g = lane >> 4, q = (lane & 15) >> 2, p = lane & 3;
  int woff[C::NT];
#pragma unroll
  for (int j = 0; j < C::NT; ++j) {
    int nn = wn * (BN / C::WGN) + 32 * j + 16 * (g & 1);
    int c = (nn >> 3) + (p >> 1);
    int kk = 8 * (g >> 1) + q;
    woff[j] = C::XB + kk * (BN * 2) + ((c ^ wswz<BN>(kk)) << 4) + (p & 1) * 8;
  }

#pragma unroll
  for (int i = 0; i < C::MT; ++i)
#pragma unroll
    for (int j = 0; j < C::NT; ++j)
#pragma unroll
      for (int r = 0; r < 16; ++r) acc[i][j][r] = 0.f;

  issue(0); issue(1); issue(2);
#pragma unroll
  for (int ks = 0; ks < 7; ++ks) {
    if (ks <= 4) wait_vm<2 * CNT>();
    else if (ks == 5) wait_vm<CNT>();
    else wait_vm<0>();
    raw_barrier();
    if (ks + 3 <= 6) issue(ks + 3);
    const char* sb = smem + (ks % C::NST) * C::STB;
#pragma unroll
    for (int k16 = 0; k16 < 4; ++k16) {
      bf16x8 xa[C::MT], wb[C::NT];
#pragma unroll
      for (int i = 0; i < C::MT; ++i)
        xa[i] = *(const bf16x8*)(sb + xoff[i] + (((2 * k16 + hh) ^ xsw) << 4));
#pragma unroll
      for (int j = 0; j < C::NT; ++j) {
        s16x4 lo = tr_read(sb + woff[j] + (16 * k16) * (BN * 2));
        s16x4 hi = tr_read(sb + woff[j] + (16 * k16 + 4) * (BN * 2));
        wb[j] = __builtin_shufflevector(lo, hi, 0, 1, 2, 3, 4, 5, 6, 7);
      }
#pragma unroll
      for (int i = 0; i < C::MT; ++i)
#pragma unroll
        for (int j = 0; j < C::NT; ++j)
          acc[i][j] = __builtin_amdgcn_mfma_f32_32x32x16_bf16(wb[j], xa[i], acc[i][j], 0, 0, 0);
    }
  }
  raw_barrier();
#pragma unroll
  for (int i = 0; i < C::MT; ++i)
#pragma unroll
    for (int j = 0; j < C::NT; ++j)
#pragma unroll
      for (int gq = 0; gq < 4; ++gq) {
        int pos = wm * (BM / C::WGM) + 32 * i + (lane & 31);
        int n = wn * (BN / C::WGN) + 32 * j + 8 * gq + 4 * hh;
        u32x2 v = {pack_bf16(acc[i][j][4 * gq] * oscale, acc[i][j][4 * gq + 1] * oscale),
                   pack_bf16(acc[i][j][4 * gq + 2] * oscale, acc[i][j][4 * gq + 3] * oscale)};
        *(u32x2*)(smem + pos * C::CROW + n * 2) = v;
      }
  raw_barrier();
}

template <int BM, int BN>
DI void store_tile(const char* smem, u16* __restrict__ dst, int ldd, int nvalid) {
  using C = GemmCfg<BM, BN>;
  constexpr int CPR = BN / 8;
  constexpr int TOT = BM * CPR;
#pragma unroll
  for (int it = 0; it < TOT / (64 * GNW); ++it) {
    int id = it * (64 * GNW) + threadIdx.x;
    int row = id / CPR, c = id % CPR;
    u32x4 v = *(const u32x4*)(smem + row * C::CROW + c * 16);
    if (c * 8 < nvalid) *(u32x4*)(dst + (size_t)row * ldd + c * 8) = v;
  }
}

__global__ __launch_bounds__(64 * GNW, 2) void proj_kernel(
    const u16* __restrict__ xA, const u16* __restrict__ wq, const u16* __restrict__ wk,
    const u16* __restrict__ wv, const u16* __restrict__ xB, const u16* __restrict__ wqkv,
    char* __restrict__ ws) {
  constexpr int BM = 128, BN = 128;
  using C = GemmCfg<BM, BN>;
  __shared__ __attribute__((aligned(16))) char smem[C::NST * C::STB];
  const int mt = blockIdx.x & 15, nt = blockIdx.x >> 4;
  const int m0 = mt * BM;
  if (nt < 4) {
    gemm_main<BM, BN, false>(xA + (size_t)m0 * KDIM, wq, D_, nt * BN, D_, nullptr, nullptr, smem, SCALE_F * LOG2E);
    store_tile<BM, BN>(smem, (u16*)(ws + WS_QA) + (size_t)m0 * D_ + nt * BN, D_, D_ - nt * BN);
  } else if (nt == 4) {
    gemm_main<BM, 64, true>(xA + (size_t)m0 * KDIM, nullptr, 0, 0, 0, wk, wv, smem);
    constexpr int CR = GemmCfg<BM, 64>::CROW;
    char* kv = ws + WS_KV + (size_t)(m0 >> 5) * 4096;
    {
      int id = threadIdx.x;
      int pos = id >> 2, c = id & 3;
      u32x4 v = *(const u32x4*)(smem + pos * CR + c * 16);
      if (c == 3) { v[2] = 0; v[3] = 0; }
      int t = pos >> 5, s = c >> 1, hh = c & 1;
      *(u32x4*)(kv + t * 4096 + s * 1024 + ((pos & 31) + 32 * hh) * 16) = v;
    }
    {
      int id = threadIdx.x;
      int t = id >> 7, s = (id >> 6) & 1, ln = id & 63;
      int d = ln & 31, hh = ln >> 5;
      u16 e[8];
#pragma unroll
      for (int j = 0; j < 8; ++j) {
        int key = 32 * t + 16 * s + 8 * (j >> 2) + 4 * hh + (j & 3);
        u16 val = *(const u16*)(smem + key * CR + (32 + d) * 2);
        if (d == 28) val = 0x3F80;
        else if (d > 28) val = 0;
        e[j] = val;
      }
      u32x4 v = {(unsigned)e[0] | ((unsigned)e[1] << 16), (unsigned)e[2] | ((unsigned)e[3] << 16),
                 (unsigned)e[4] | ((unsigned)e[5] << 16), (unsigned)e[6] | ((unsigned)e[7] << 16)};
      *(u32x4*)(kv + t * 4096 + 2048 + s * 1024 + ln * 16) = v;
    }
  } else {
    const int n0 = (nt - 5) * BN;
    gemm_main<BM, BN, false>(xB + (size_t)m0 * KDIM, wqkv, 3 * D_, n0, 3 * D_, nullptr, nullptr, smem);
    store_tile<BM, BN>(smem, (u16*)(ws + WS_QKVB) + (size_t)m0 * 3 * D_ + n0, 3 * D_, 3 * D_ - n0);
  }
}

__global__ __launch_bounds__(64 * GNW, 2) void outproj_kernel(
    const char* __restrict__ ws, const u16* __restrict__ woA, const u16* __restrict__ woB,
    u16* __restrict__ out) {
  constexpr int BM = 128, BN = 64;
  using C = GemmCfg<BM, BN>;
  __shared__ __attribute__((aligned(16))) char smem[C::NST * C::STB];
  const int mt = blockIdx.x & 15, r = blockIdx.x >> 4;
  const int br = r >= 7, nt = br ? r - 7 : r;
  const int m0 = mt * BM;
  const u16* X = (const u16*)(ws + (br ? WS_OB : WS_OA)) + (size_t)m0 * KDIM;
  gemm_main<BM, BN, false>(X, br ? woB : woA, D_, nt * BN, D_, nullptr, nullptr, smem);
  store_tile<BM, BN>(smem, out + (size_t)m0 * (2 * D_) + br * D_ + nt * BN, 2 * D_, BN);
}

DI float dpp_xor1(float x) {
  return __uint_as_float(__builtin_amdgcn_update_dpp(0, __float_as_uint(x), 0xB1, 0xf, 0xf, true));
}
DI float dpp_xor2(float x) {
  return __uint_as_float(__builtin_amdgcn_update_dpp(0, __float_as_uint(x), 0x4E, 0xf, 0xf, true));
}

DI float dpp_hmir(float x) {
  return __uint_as_float(__builtin_amdgcn_update_dpp(0, __float_as_uint(x), 0x141, 0xf, 0xf, true));
}
DI void ld4(const u16* p, float& a, float& b, float& c, float& d) {
  u32x2 v = *(const u32x2*)p;
  a = bf_lo(v[0]); b = bf_hi(v[0]); c = bf_lo(v[1]); d = bf_hi(v[1]);
}

constexpr int BSL = 2064;
constexpr int BWB = 18 * 1024;
DI void local_attn_item(const char* img, u16* __restrict__ oB, int pos, int h, int j, int lane) {
  const char* qp = img + h * 56;
  const char* kp = img + 1024 + j * BSL + h * 56;
  u32x2 qv[7], kv[7], vv[8];
#pragma unroll
  for (int i = 0; i < 7; ++i) { qv[i] = *(const u32x2*)(qp + 8 * i); kv[i] = *(const u32x2*)(kp + 8 * i); }
  const int jj = j < 7 ? j : 6;
#pragma unroll
  for (int wdw = 0; wdw < 8; ++wdw)
    vv[wdw] = *(const u32x2*)(img + 1024 + wdw * BSL + 896 + h * 56 + 8 * jj);
  float sa = 0.f;
#pragma unroll
  for (int i = 0; i < 7; ++i) {
    sa = fmaf(bf_lo(qv[i][0]), bf_lo(kv[i][0]), sa);
    sa = fmaf(bf_hi(qv[i][0]), bf_hi(kv[i][0]), sa);
    sa = fmaf(bf_lo(qv[i][1]), bf_lo(kv[i][1]), sa);
    sa = fmaf(bf_hi(qv[i][1]), bf_hi(kv[i][1]), sa);
  }
  float la = rnd_bf16(rnd_bf16(sa) * SCALE_F);
  if (pos - 2 * j < 0) la = -1e9f;
  float m = fmaxf(la, dpp_xor1(la));
  m = fmaxf(m, dpp_xor2(m));
  m = fmaxf(m, dpp_hmir(m));
  float e = __builtin_amdgcn_exp2f((la - m) * LOG2E);
  float sum = e + dpp_xor1(e);
  sum += dpp_xor2(sum);
  sum += dpp_hmir(sum);
  float pj_ = rnd_bf16(e / sum);
  float o0 = 0.f, o1 = 0.f, o2 = 0.f, o3 = 0.f;
#pragma unroll
  for (int wdw = 0; wdw < 8; ++wdw) {
    float pw = __shfl(pj_, (lane & ~7) | wdw);
    o0 = fmaf(pw, bf_lo(vv[wdw][0]), o0); o1 = fmaf(pw, bf_hi(vv[wdw][0]), o1);
    o2 = fmaf(pw, bf_lo(vv[wdw][1]), o2); o3 = fmaf(pw, bf_hi(vv[wdw][1]), o3);
  }
  if (j < 7) {
    u32x2 v0 = {pack_bf16(o0, o1), pack_bf16(o2, o3)};
    *(u32x2*)(oB + (size_t)pos * D_ + h * DH + 4 * j) = v0;
  }
}

constexpr float PTHR = 1.2379400392853803e27f;
constexpr float FMAXF = 3.0e38f;

DI void pv_tile(f32x16& O, const f32x16& s, bf16x8 v0, bf16x8 v1) {
  float p[16];
#pragma unroll
  for (int r = 0; r < 16; ++r) p[r] = __builtin_amdgcn_exp2f(s[r]);
  u32x4 pa = {pack_bf16(p[0], p[1]), pack_bf16(p[2], p[3]), pack_bf16(p[4], p[5]), pack_bf16(p[6], p[7])};
  u32x4 pbv = {pack_bf16(p[8], p[9]), pack_bf16(p[10], p[11]), pack_bf16(p[12], p[13]), pack_bf16(p[14], p[15])};
  O = __builtin_amdgcn_mfma_f32_32x32x16_bf16(v0, __builtin_bit_cast(bf16x8, pa), O, 0, 0, 0);
  O = __builtin_amdgcn_mfma_f32_32x32x16_bf16(v1, __builtin_bit_cast(bf16x8, pbv), O, 0, 0, 0);
}
DI void careful_tile(f32x16& O, f32x16& C, float& R, bool first, bf16x8 k0, bf16x8 k1, bf16x8 v0, bf16x8 v1,
                     bf16x8 q0, bf16x8 q1) {
  f32x16 s = __builtin_amdgcn_mfma_f32_32x32x16_bf16(k0, q0, C, 0, 0, 0);
  s = __builtin_amdgcn_mfma_f32_32x32x16_bf16(k1, q1, s, 0, 0, 0);
  float tm = fmaxf(s[0], s[1]);
#pragma unroll
  for (int r = 2; r < 16; r += 2) tm = fmaxf(fmaxf(tm, s[r]), s[r + 1]);
  auto rr = __builtin_amdgcn_permlane32_swap(__float_as_uint(tm), __float_as_uint(tm), false, false);
  float delta = fmaxf(__uint_as_float(rr[0]), __uint_as_float(rr[1]));
  if (!first) {
    delta = fmaxf(delta, 0.f);
    float alpha = __builtin_amdgcn_exp2f(-delta);
#pragma unroll
    for (int r = 0; r < 16; ++r) O[r] *= alpha;
  }
  R += delta;
#pragma unroll
  for (int r = 0; r < 16; ++r) { s[r] -= delta; C[r] = -R; }
  pv_tile(O, s, v0, v1);
}

__global__ __launch_bounds__(512, 2) void attn_kernel(char* __restrict__ ws) {
  __shared__ __attribute__((aligned(16))) char smem[8 * BWB];
  const int lane = threadIdx.x & 63;
  const int w = __builtin_amdgcn_readfirstlane(threadIdx.x >> 6);
  const int hh = lane >> 5;
  const int head = lane & 15;
  const int p0 = blockIdx.x * 8;
  const char* kvp = ws + WS_KV + (size_t)(w * 8) * 4096 + lane * 16;

  bf16x8 k0 = *(const bf16x8*)(kvp), k1 = *(const bf16x8*)(kvp + 1024);
  bf16x8 v0 = *(const bf16x8*)(kvp + 2048), v1 = *(const bf16x8*)(kvp + 3072);

  bf16x8 q0[4], q1[4];
#pragma unroll
  for (int b = 0; b < 4; ++b) {
    const int pos = p0 + b * 2 + ((lane >> 4) & 1);
    const u16* qp = (const u16*)(ws + WS_QA) + (size_t)pos * D_ + head * DH + 8 * hh;
    u32x2 a = *(const u32x2*)(qp), bb = *(const u32x2*)(qp + 4);
    u32x2 c = *(const u32x2*)(qp + 16);
    u32x2 d = {0u, 0u};
    if (hh == 0) d = *(const u32x2*)(qp + 20);
    u32x4 x0 = {a[0], a[1], bb[0], bb[1]}, x1 = {c[0], c[1], d[0], d[1]};
    q0[b] = __builtin_bit_cast(bf16x8, x0);
    q1[b] = __builtin_bit_cast(bf16x8, x1);
  }
  auto load_bimg = [&](int pos) {
    const char* qkvb = ws + WS_QKVB;
#pragma unroll
    for (int pc = 0; pc < 18; ++pc) {
      int off = pc * 1024 + lane * 16;
      const char* src;
      if (pc == 0) {
        src = qkvb + (size_t)pos * 2688 + (off < 896 ? off : 0);
      } else {
        int o2 = off - 1024;
        int j = o2 / BSL, col = o2 - j * BSL;
        if (j > 7) j = 7;
        if (col >= 1792) col = 0;
        int pr = pos - 2 * j; if (pr < 0) pr = 0;
        src = qkvb + (size_t)pr * 2688 + 896 + col;
      }
      glds16(src, smem + w * BWB + pc * 1024);
    }
  };

  if (w < 4) load_bimg(p0 + w);
  __builtin_amdgcn_s_waitcnt(0x0F70);
  f32x16 O[4], C[4];
  float R[4];
#pragma unroll
  for (int b = 0; b < 4; ++b) {
    R[b] = 0.f;
#pragma unroll
    for (int r = 0; r < 16; ++r) { O[b][r] = 0.f; C[b][r] = 0.f; }
  }
  {
    bf16x8 nk0 = *(const bf16x8*)(kvp + 4096), nk1 = *(const bf16x8*)(kvp + 4096 + 1024);
    bf16x8 nv0 = *(const bf16x8*)(kvp + 4096 + 2048), nv1 = *(const bf16x8*)(kvp + 4096 + 3072);
#pragma unroll
    for (int b = 0; b < 4; ++b) careful_tile(O[b], C[b], R[b], true, k0, k1, v0, v1, q0[b], q1[b]);
    k0 = nk0; k1 = nk1; v0 = nv0; v1 = nv1;
  }
  asm volatile("" : "+v"(k0), "+v"(k1), "+v"(v0), "+v"(v1));
  for (int t = 1; t < 8; ++t) {
    const int tn = t < 7 ? t + 1 : 7;
    bf16x8 nk0, nk1, nv0, nv1;
    {
      const char* np = kvp + tn * 4096;
      asm volatile("global_load_dwordx4 %0, %4, off\n\tglobal_load_dwordx4 %1, %4, off offset:1024\n\t"
                   "global_load_dwordx4 %2, %4, off offset:2048\n\tglobal_load_dwordx4 %3, %4, off offset:3072"
                   : "=&v"(nk0), "=&v"(nk1), "=&v"(nv0), "=&v"(nv1) : "v"(np) : "memory");
    }
    __builtin_amdgcn_sched_barrier(0);
#pragma unroll
    for (int bp = 0; bp < 2; ++bp) {
      f32x16 sa = __builtin_amdgcn_mfma_f32_32x32x16_bf16(k0, q0[2 * bp], C[2 * bp], 0, 0, 0);
      f32x16 sb = __builtin_amdgcn_mfma_f32_32x32x16_bf16(k0, q0[2 * bp + 1], C[2 * bp + 1], 0, 0, 0);
      sa = __builtin_amdgcn_mfma_f32_32x32x16_bf16(k1, q1[2 * bp], sa, 0, 0, 0);
      sb = __builtin_amdgcn_mfma_f32_32x32x16_bf16(k1, q1[2 * bp + 1], sb, 0, 0, 0);
      pv_tile(O[2 * bp], sa, v0, v1);
      pv_tile(O[2 * bp + 1], sb, v0, v1);
      __builtin_amdgcn_sched_barrier(0);
    }
    asm volatile("s_waitcnt vmcnt(0)" : "+v"(nk0), "+v"(nk1), "+v"(nv0), "+v"(nv1) :: "memory");
    k0 = nk0; k1 = nk1; v0 = nv0; v1 = nv1;
  }
  {
    float lm = fmaxf(fmaxf(O[0][12], O[1][12]), fmaxf(O[2][12], O[3][12]));
    float z = 0.f;
#pragma unroll
    for (int b = 0; b < 4; ++b)
#pragma unroll
      for (int r = 0; r < 16; ++r) z = fmaf(O[b][r], 0.f, z);
    bool bad = ((hh == 1) && !(lm <= PTHR)) || !(z == 0.f);
    if (__builtin_expect(__any(bad), 0)) {
#pragma unroll
      for (int b = 0; b < 4; ++b) {
        R[b] = 0.f;
#pragma unroll
        for (int r = 0; r < 16; ++r) { O[b][r] = 0.f; C[b][r] = 0.f; }
      }
#pragma unroll 1
      for (int t = 0; t < 8; ++t) {
        bf16x8 a0 = *(const bf16x8*)(kvp + t * 4096), a1 = *(const bf16x8*)(kvp + t * 4096 + 1024);
        bf16x8 b0 = *(const bf16x8*)(kvp + t * 4096 + 2048), b1 = *(const bf16x8*)(kvp + t * 4096 + 3072);
#pragma unroll
        for (int b = 0; b < 4; ++b) careful_tile(O[b], C[b], R[b], t == 0, a0, a1, b0, b1, q0[b], q1[b]);
      }
    }
  }

  if (w < 4) {
    WAIT_VM(0);
    local_attn_item(smem + w * BWB, (u16*)(ws + WS_OB), p0 + w, lane >> 3, lane & 7, lane);
    local_attn_item(smem + w * BWB, (u16*)(ws + WS_OB), p0 + w, 8 + (lane >> 3), lane & 7, lane);
    asm volatile("s_waitcnt lgkmcnt(0)" ::: "memory");
    load_bimg(p0 + w + 4);
    WAIT_VM(0);
    local_attn_item(smem + w * BWB, (u16*)(ws + WS_OB), p0 + w + 4, lane >> 3, lane & 7, lane);
    local_attn_item(smem + w * BWB, (u16*)(ws + WS_OB), p0 + w + 4, 8 + (lane >> 3), lane & 7, lane);
    asm volatile("s_waitcnt lgkmcnt(0)" ::: "memory");
  }

  {
    typedef float f32x4 __attribute__((ext_vector_type(4)));
    f32x4* xo = (f32x4*)(smem + w * BWB);
    float* xr = (float*)(smem + w * BWB + 16384);
#pragma unroll
    for (int b = 0; b < 4; ++b) {
#pragma unroll
      for (int g = 0; g < 4; ++g)
        xo[(b * 4 + g) * 64 + lane] = (f32x4){O[b][4 * g], O[b][4 * g + 1], O[b][4 * g + 2], O[b][4 * g + 3]};
      xr[b * 64 + lane] = R[b];
    }
  }
  raw_barrier();
  {
    typedef float f32x4 __attribute__((ext_vector_type(4)));
    const int b = w >> 1;
    float Rs[8], a[8];
#pragma unroll
    for (int i = 0; i < 8; ++i) Rs[i] = ((const float*)(smem + i * BWB + 16384))[b * 64 + lane];
    float Rn = Rs[0];
#pragma unroll
    for (int i = 1; i < 8; ++i) Rn = fmaxf(Rn, Rs[i]);
    float l = 0.f;
#pragma unroll
    for (int i = 0; i < 8; ++i) {
      a[i] = __builtin_amdgcn_exp2f(Rs[i] - Rn);
      l = fmaf(((const f32x4*)(smem + i * BWB))[(b * 4 + 3) * 64 + (lane & 31) + 32][0], a[i], l);
    }
    const float inv = 1.0f / l;
    const int pos = p0 + b * 2 + ((lane >> 4) & 1);
#pragma unroll
    for (int gg = 0; gg < 2; ++gg) {
      const int g = (w & 1) * 2 + gg;
      f32x4 o = {0.f, 0.f, 0.f, 0.f};
#pragma unroll
      for (int i = 0; i < 8; ++i) {
        f32x4 x = ((const f32x4*)(smem + i * BWB))[(b * 4 + g) * 64 + lane];
        o[0] = fmaf(x[0], a[i], o[0]); o[1] = fmaf(x[1], a[i], o[1]);
        o[2] = fmaf(x[2], a[i], o[2]); o[3] = fmaf(x[3], a[i], o[3]);
      }
      if (g < 3 || hh == 0) {
        u32x2 v = {pack_bf16(o[0] * inv, o[1] * inv), pack_bf16(o[2] * inv, o[3] * inv)};
        *(u32x2*)((u16*)(ws + WS_OA) + (size_t)pos * D_ + head * DH + 4 * hh + 8 * g) = v;
      }
    }
  }
}

extern "C" void kernel_launch(void* const* d_in, const int* in_sizes, int n_in,
                              void* d_out, int out_size, void* d_ws, size_t ws_size,
                              hipStream_t stream) {
  const u16* xA = (const u16*)d_in[0];
  const u16* wq = (const u16*)d_in[1];
  const u16* wk = (const u16*)d_in[2];
  const u16* wv = (const u16*)d_in[3];
  const u16* woA = (const u16*)d_in[4];
  const u16* xB = (const u16*)d_in[5];
  const u16* wqkv = (const u16*)d_in[6];
  const u16* woB = (const u16*)d_in[7];
  char* ws = (char*)d_ws;
  const int g_mask = 0xff;
  if (g_mask & 1) proj_kernel<<<256, 64 * GNW, 0, stream>>>(xA, wq, wk, wv, xB, wqkv, ws);
  if (g_mask & 2) attn_kernel<<<256, 512, 0, stream>>>(ws);
  if (g_mask & 4) outproj_kernel<<<224, 64 * GNW, 0, stream>>>(ws, woA, woB, (u16*)d_out);
}
```

```cpp
#include <hip/hip_runtime.h>
#include <hip/hip_bf16.h>
#include <stdint.h>

constexpr int S_ = 2048, D_ = 448, H_ = 16, DH = 28;
constexpr int KDIM = 448;
constexpr float SCALE_F = 0.18898223650461362f;
constexpr float LOG2E = 1.4426950408889634f;

typedef unsigned short u16;
typedef short bf16x8 __attribute__((ext_vector_type(8)));
typedef short s16x4 __attribute__((ext_vector_type(4)));
typedef float f32x16 __attribute__((ext_vector_type(16)));
typedef float f32x2 __attribute__((ext_vector_type(2)));
typedef __bf16 bf16x2_t __attribute__((ext_vector_type(2)));
typedef unsigned u32x4 __attribute__((ext_vector_type(4)));
typedef unsigned u32x2 __attribute__((ext_vector_type(2)));

#define DI __device__ __forceinline__

DI unsigned pack_bf16(float lo, float hi) {
  f32x2 v = {lo, hi};
  bf16x2_t b = __builtin_convertvector(v, bf16x2_t);
  return __builtin_bit_cast(unsigned, b);
}
DI float rnd_bf16(float x) { return __uint_as_float(pack_bf16(0.f, x)); }
DI float bf_lo(unsigned u) { return __uint_as_float(u << 16); }
DI float bf_hi(unsigned u) { return __uint_as_float(u & 0xffff0000u); }

#define WAIT_VM(n) asm volatile("s_waitcnt vmcnt(" #n ")" ::: "memory")
template <int N> DI void wait_vm() { asm volatile("s_waitcnt vmcnt(%0)" ::"n"(N) : "memory"); }
DI void raw_barrier() {
  asm volatile("s_waitcnt lgkmcnt(0)" ::: "memory");
  __builtin_amdgcn_s_barrier();
}

DI void glds16(const void* g, void* l) {
  __builtin_amdgcn_global_load_lds((const unsigned*)g, (unsigned*)l, 16, 0, 0);
}
DI void glds4(const void* g, void* l) {
  __builtin_amdgcn_global_load_lds((const unsigned*)g, (unsigned*)l, 4, 0, 0);
}
DI s16x4 tr_read(const void* l) {
  return __builtin_amdgcn_ds_read_tr16_b64_v4i16((__attribute__((address_space(3))) s16x4*)l);
}

constexpr size_t WS_QA = 0;
constexpr size_t WS_KV = WS_QA + (size_t)S_ * D_ * 2;
constexpr size_t WS_QKVB = WS_KV + 64 * 4096;
constexpr size_t WS_OA = WS_QKVB + (size_t)S_ * 3 * D_ * 2;
constexpr size_t WS_OB = WS_OA + (size_t)S_ * D_ * 2;

constexpr int GNW = 8;
template <int BM, int BN> struct GemmCfg {
  static constexpr int XB = BM * 128;
  static constexpr int WB = 64 * BN * 2;
  static constexpr int STB = XB + WB;
  static constexpr int NST = 4;
  static constexpr int XI = XB / 1024 / GNW;
  static constexpr int WI = WB / 1024 / GNW;
  static constexpr int WGM = (BM >= BN) ? 4 : 2, WGN = GNW / WGM;
  static constexpr int MT = BM / 32 / WGM, NT = BN / 32 / WGN;
  static constexpr int CROW = BN * 2 + 16;
};

template <int BN> DI int wswz(int k) {
  if constexpr (BN == 128) return (k & 3) << 2;
  else return ((k >> 1) & 1) << 2;
}

template <int BM, int BN, bool KV>
DI void gemm_main(const u16* __restrict__ X,
                  const u16* __restrict__ W, int ldw, int n0, int ncols,
                  const u16* __restrict__ Wk, const u16* __restrict__ Wv,
                  char* smem, float oscale = 1.0f) {
  using C = GemmCfg<BM, BN>;
  const int lane = threadIdx.x & 63;
  const int w = __builtin_amdgcn_readfirstlane(threadIdx.x >> 6);
  const int wm = w / C::WGN, wn = w % C::WGN;
  f32x16 acc[C::MT][C::NT];
  constexpr int KVI = 32 / GNW;
  constexpr int WCNT = KV ? KVI : C::WI;
  constexpr int CNT = C::XI + WCNT;

  const u16* xsrc[C::XI];
#pragma unroll
  for (int i = 0; i < C::XI; ++i) {
    int row = (w * C::XI + i) * 8 + (lane >> 3);
    int c = (lane & 7) ^ ((row >> 1) & 7);
    xsrc[i] = X + row * KDIM + c * 8;
  }
  const u16* wsrc[WCNT];
  if constexpr (!KV) {
    constexpr int CPR = BN / 8, RPI = 1024 / (BN * 2);
#pragma unroll
    for (int i = 0; i < C::WI; ++i) {
      int k = (w * C::WI + i) * RPI + lane / CPR;
      int c = (lane % CPR) ^ wswz<BN>(k);
      int col = n0 + c * 8;
      if (col + 8 > ncols) col = ncols - 8;
      wsrc[i] = W + k * ldw + col;
    }
  } else {
#pragma unroll
    for (int i = 0; i < KVI; ++i) {
      int k = (w * KVI + i) * 2 + (lane >> 5);
      int c = ((lane & 31) >> 2) ^ wswz<BN>(k);
      int col = c * 8 + (lane & 3) * 2;
      const u16* p = Wk + k * DH;
      if (col < 28) p = Wk + k * DH + col;
      else if (col >= 32 && col < 60) p = Wv + k * DH + (col - 32);
      wsrc[i] = p;
    }
  }
  auto issue = [&](int ks) {
    char* sb = smem + (ks % C::NST) * C::STB;
#pragma unroll
    for (int i = 0; i < C::XI; ++i) glds16(xsrc[i] + ks * 64, sb + (w * C::XI + i) * 1024);
    if constexpr (!KV) {
#pragma unroll
      for (int i = 0; i < C::WI; ++i) glds16(wsrc[i] + (size_t)ks * 64 * ldw, sb + C::XB + (w * C::WI + i) * 1024);
    } else {
#pragma unroll
      for (int i = 0; i < KVI; ++i) glds4(wsrc[i] + ks * 64 * DH, sb + C::XB + (w * KVI + i) * 256);
    }
  };

  const int hh = lane >> 5;
  int xoff[C::MT];
#pragma unroll
  for (int i = 0; i < C::MT; ++i) {
    int row = wm * (BM / C::WGM) + 32 * i + (lane & 31);
    xoff[i] = row * 128;
  }
  const int xsw = ((lane & 31) >> 1) & 7;
  const int g = lane >> 4, q = (lane & 15) >> 2, p = lane & 3;
  int woff[C::NT];
#pragma unroll
  for (int j = 0; j < C::NT; ++j) {
    int nn = wn * (BN / C::WGN) + 32 * j + 16 * (g & 1);
    int c = (nn >> 3) + (p >> 1);
    int kk = 8 * (g >> 1) + q;
    woff[j] = C::XB + kk * (BN * 2) + ((c ^ wswz<BN>(kk)) << 4) + (p & 1) * 8;
  }

#pragma unroll
  for (int i = 0; i < C::MT; ++i)
#pragma unroll
    for (int j = 0; j < C::NT; ++j)
#pragma unroll
      for (int r = 0; r < 16; ++r) acc[i][j][r] = 0.f;

  issue(0); issue(1); issue(2);
#pragma unroll
  for (int ks = 0; ks < 7; ++ks) {
    if (ks <= 4) wait_vm<2 * CNT>();
    else if (ks == 5) wait_vm<CNT>();
    else wait_vm<0>();
    raw_barrier();
    if (ks + 3 <= 6) issue(ks + 3);
    const char* sb = smem + (ks % C::NST) * C::STB;
#pragma unroll
    for (int k16 = 0; k16 < 4; ++k16) {
      bf16x8 xa[C::MT], wb[C::NT];
#pragma unroll
      for (int i = 0; i < C::MT; ++i)
        xa[i] = *(const bf16x8*)(sb + xoff[i] + (((2 * k16 + hh) ^ xsw) << 4));
#pragma unroll
      for (int j = 0; j < C::NT; ++j) {
        s16x4 lo = tr_read(sb + woff[j] + (16 * k16) * (BN * 2));
        s16x4 hi = tr_read(sb + woff[j] + (16 * k16 + 4) * (BN * 2));
        wb[j] = __builtin_shufflevector(lo, hi, 0, 1, 2, 3, 4, 5, 6, 7);
      }
#pragma unroll
      for (int i = 0; i < C::MT; ++i)
#pragma unroll
        for (int j = 0; j < C::NT; ++j)
          acc[i][j] = __builtin_amdgcn_mfma_f32_32x32x16_bf16(wb[j], xa[i], acc[i][j], 0, 0, 0);
    }
  }
  raw_barrier();
#pragma unroll
  for (int i = 0; i < C::MT; ++i)
#pragma unroll
    for (int j = 0; j < C::NT; ++j)
#pragma unroll
      for (int gq = 0; gq < 4; ++gq) {
        int pos = wm * (BM / C::WGM) + 32 * i + (lane & 31);
        int n = wn * (BN / C::WGN) + 32 * j + 8 * gq + 4 * hh;
        u32x2 v = {pack_bf16(acc[i][j][4 * gq] * oscale, acc[i][j][4 * gq + 1] * oscale),
                   pack_bf16(acc[i][j][4 * gq + 2] * oscale, acc[i][j][4 * gq + 3] * oscale)};
        *(u32x2*)(smem + pos * C::CROW + n * 2) = v;
      }
  raw_barrier();
}

template <int BM, int BN>
DI void store_tile(const char* smem, u16* __restrict__ dst, int ldd, int nvalid) {
  using C = GemmCfg<BM, BN>;
  constexpr int CPR = BN / 8;
  constexpr int TOT = BM * CPR;
#pragma unroll
  for (int it = 0; it < TOT / (64 * GNW); ++it) {
    int id = it * (64 * GNW) + threadIdx.x;
    int row = id / CPR, c = id % CPR;
    u32x4 v = *(const u32x4*)(smem + row * C::CROW + c * 16);
    if (c * 8 < nvalid) *(u32x4*)(dst + (size_t)row * ldd + c * 8) = v;
  }
}

__global__ __launch_bounds__(64 * GNW, 2) void proj_kernel(
    const u16* __restrict__ xA, const u16* __restrict__ wq, const u16* __restrict__ wk,
    const u16* __restrict__ wv, const u16* __restrict__ xB, const u16* __restrict__ wqkv,
    char* __restrict__ ws) {
  constexpr int BM = 128, BN = 128;
  using C = GemmCfg<BM, BN>;
  __shared__ __attribute__((aligned(16))) char smem[C::NST * C::STB];
  const int mt = blockIdx.x & 15, nt = blockIdx.x >> 4;
  const int m0 = mt * BM;
  if (nt < 4) {
    gemm_main<BM, BN, false>(xA + (size_t)m0 * KDIM, wq, D_, nt * BN, D_, nullptr, nullptr, smem, SCALE_F * LOG2E);
    store_tile<BM, BN>(smem, (u16*)(ws + WS_QA) + (size_t)m0 * D_ + nt * BN, D_, D_ - nt * BN);
  } else if (nt == 4) {
    gemm_main<BM, 64, true>(xA + (size_t)m0 * KDIM, nullptr, 0, 0, 0, wk, wv, smem);
    constexpr int CR = GemmCfg<BM, 64>::CROW;
    char* kv = ws + WS_KV + (size_t)(m0 >> 5) * 4096;
    {
      int id = threadIdx.x;
      int pos = id >> 2, c = id & 3;
      u32x4 v = *(const u32x4*)(smem + pos * CR + c * 16);
      if (c == 3) { v[2] = 0; v[3] = 0; }
      int t = pos >> 5, s = c >> 1, hh = c & 1;
      *(u32x4*)(kv + t * 4096 + s * 1024 + ((pos & 31) + 32 * hh) * 16) = v;
    }
    {
      int id = threadIdx.x;
      int t = id >> 7, s = (id >> 6) & 1, ln = id & 63;
      int d = ln & 31, hh = ln >> 5;
      u16 e[8];
#pragma unroll
      for (int j = 0; j < 8; ++j) {
        int key = 32 * t + 16 * s + 8 * (j >> 2) + 4 * hh + (j & 3);
        u16 val = *(const u16*)(smem + key * CR + (32 + d) * 2);
        if (d == 28) val = 0x3F80;
        else if (d > 28) val = 0;
        e[j] = val;
      }
      u32x4 v = {(unsigned)e[0] | ((unsigned)e[1] << 16), (unsigned)e[2] | ((unsigned)e[3] << 16),
                 (unsigned)e[4] | ((unsigned)e[5] << 16), (unsigned)e[6] | ((unsigned)e[7] << 16)};
      *(u32x4*)(kv + t * 4096 + 2048 + s * 1024 + ln * 16) = v;
    }
  } else {
    const int n0 = (nt - 5) * BN;
    gemm_main<BM, BN, false>(xB + (size_t)m0 * KDIM, wqkv, 3 * D_, n0, 3 * D_, nullptr, nullptr, smem);
    store_tile<BM, BN>(smem, (u16*)(ws + WS_QKVB) + (size_t)m0 * 3 * D_ + n0, 3 * D_, 3 * D_ - n0);
  }
}

__global__ __launch_bounds__(64 * GNW, 2) void outproj_kernel(
    const char* __restrict__ ws, const u16* __restrict__ woA, const u16* __restrict__ woB,
    u16* __restrict__ out) {
  constexpr int BM = 128, BN = 64;
  using C = GemmCfg<BM, BN>;
  __shared__ __attribute__((aligned(16))) char smem[C::NST * C::STB];
  const int mt = blockIdx.x & 15, r = blockIdx.x >> 4;
  const int br = r >= 7, nt = br ? r - 7 : r;
  const int m0 = mt * BM;
  const u16* X = (const u16*)(ws + (br ? WS_OB : WS_OA)) + (size_t)m0 * KDIM;
  gemm_main<BM, BN, false>(X, br ? woB : woA, D_, nt * BN, D_, nullptr, nullptr, smem);
  store_tile<BM, BN>(smem, out + (size_t)m0 * (2 * D_) + br * D_ + nt * BN, 2 * D_, BN);
}

DI float dpp_xor1(float x) {
  return __uint_as_float(__builtin_amdgcn_update_dpp(0, __float_as_uint(x), 0xB1, 0xf, 0xf, true));
}
DI float dpp_xor2(float x) {
  return __uint_as_float(__builtin_amdgcn_update_dpp(0, __float_as_uint(x), 0x4E, 0xf, 0xf, true));
}

DI float dpp_hmir(float x) {
  return __uint_as_float(__builtin_amdgcn_update_dpp(0, __float_as_uint(x), 0x141, 0xf, 0xf, true));
}
constexpr int BSL = 2064;
constexpr int BWB = 18 * 1024;
DI void local_attn_item(const char* img, u16* __restrict__ oB, int pos, int h, int j, int lane) {
  const char* qp = img + h * 56;
  const char* kp = img + 1024 + j * BSL + h * 56;
  u32x2 qv[7], kv[7], vv[8];
#pragma unroll
  for (int i = 0; i < 7; ++i) { qv[i] = *(const u32x2*)(qp + 8 * i); kv[i] = *(const u32x2*)(kp + 8 * i); }
  const int jj = j < 7 ? j : 6;
#pragma unroll
  for (int wdw = 0; wdw < 8; ++wdw)
    vv[wdw] = *(const u32x2*)(img + 1024 + wdw * BSL + 896 + h * 56 + 8 * jj);
  float sa = 0.f;
#pragma unroll
  for (int i = 0; i < 7; ++i) {
    sa = fmaf(bf_lo(qv[i][0]), bf_lo(kv[i][0]), sa);
    sa = fmaf(bf_hi(qv[i][0]), bf_hi(kv[i][0]), sa);
    sa = fmaf(bf_lo(qv[i][1]), bf_lo(kv[i][1]), sa);
    sa = fmaf(bf_hi(qv[i][1]), bf_hi(kv[i][1]), sa);
  }
  float la = rnd_bf16(rnd_bf16(sa) * SCALE_F);
  if (pos - 2 * j < 0) la = -1e9f;
  float m = fmaxf(la, dpp_xor1(la));
  m = fmaxf(m, dpp_xor2(m));
  m = fmaxf(m, dpp_hmir(m));
  float e = __builtin_amdgcn_exp2f((la - m) * LOG2E);
  float sum = e + dpp_xor1(e);
  sum += dpp_xor2(sum);
  sum += dpp_hmir(sum);
  float pj_ = rnd_bf16(e / sum);
  float o0 = 0.f, o1 = 0.f, o2 = 0.f, o3 = 0.f;
#pragma unroll
  for (int wdw = 0; wdw < 8; ++wdw) {
    float pw = __shfl(pj_, (lane & ~7) | wdw);
    o0 = fmaf(pw, bf_lo(vv[wdw][0]), o0); o1 = fmaf(pw, bf_hi(vv[wdw][0]), o1);
    o2 = fmaf(pw, bf_lo(vv[wdw][1]), o2); o3 = fmaf(pw, bf_hi(vv[wdw][1]), o3);
  }
  if (j < 7) {
    u32x2 v0 = {pack_bf16(o0, o1), pack_bf16(o2, o3)};
    *(u32x2*)(oB + (size_t)pos * D_ + h * DH + 4 * j) = v0;
  }
}

constexpr float PTHR = 1.2379400392853803e27f;

DI void pv_tile(f32x16& O, const f32x16& s, bf16x8 v0, bf16x8 v1) {
  float p[16];
#pragma unroll
  for (int r = 0; r < 16; ++r) p[r] = __builtin_amdgcn_exp2f(s[r]);
  u32x4 pa = {pack_bf16(p[0], p[1]), pack_bf16(p[2], p[3]), pack_bf16(p[4], p[5]), pack_bf16(p[6], p[7])};
  u32x4 pbv = {pack_bf16(p[8], p[9]), pack_bf16(p[10], p[11]), pack_bf16(p[12], p[13]), pack_bf16(p[14], p[15])};
  O = __builtin_amdgcn_mfma_f32_32x32x16_bf16(v0, __builtin_bit_cast(bf16x8, pa), O, 0, 0, 0);
  O = __builtin_amdgcn_mfma_f32_32x32x16_bf16(v1, __builtin_bit_cast(bf16x8, pbv), O, 0, 0, 0);
}
DI void careful_tile(f32x16& O, f32x16& C, float& R, bool first, bf16x8 k0, bf16x8 k1, bf16x8 v0, bf16x8 v1,
                     bf16x8 q0, bf16x8 q1) {
  f32x16 s = __builtin_amdgcn_mfma_f32_32x32x16_bf16(k0, q0, C, 0, 0, 0);
  s = __builtin_amdgcn_mfma_f32_32x32x16_bf16(k1, q1, s, 0, 0, 0);
  float tm = fmaxf(s[0], s[1]);
#pragma unroll
  for (int r = 2; r < 16; r += 2) tm = fmaxf(fmaxf(tm, s[r]), s[r + 1]);
  auto rr = __builtin_amdgcn_permlane32_swap(__float_as_uint(tm), __float_as_uint(tm), false, false);
  float delta = fmaxf(__uint_as_float(rr[0]), __uint_as_float(rr[1]));
  if (!first) {
    delta = fmaxf(delta, 0.f);
    float alpha = __builtin_amdgcn_exp2f(-delta);
#pragma unroll
    for (int r = 0; r < 16; ++r) O[r] *= alpha;
  }
  R += delta;
#pragma unroll
  for (int r = 0; r < 16; ++r) { s[r] -= delta; C[r] = -R; }
  pv_tile(O, s, v0, v1);
}

__global__ __launch_bounds__(512, 2) void attn_kernel(char* __restrict__ ws) {
  __shared__ __attribute__((aligned(16))) char smem[8 * BWB];
  const int lane = threadIdx.x & 63;
  const int w = __builtin_amdgcn_readfirstlane(threadIdx.x >> 6);
  const int hh = lane >> 5;
  const int head = lane & 15;
  const int p0 = blockIdx.x * 8;
  const char* kvp = ws + WS_KV + (size_t)(w * 8) * 4096 + lane * 16;

  bf16x8 k0 = *(const bf16x8*)(kvp), k1 = *(const bf16x8*)(kvp + 1024);
  bf16x8 v0 = *(const bf16x8*)(kvp + 2048), v1 = *(const bf16x8*)(kvp + 3072);

  bf16x8 q0[4], q1[4];
#pragma unroll
  for (int b = 0; b < 4; ++b) {
    const int pos = p0 + b * 2 + ((lane >> 4) & 1);
    const u16* qp = (const u16*)(ws + WS_QA) + (size_t)pos * D_ + head * DH + 8 * hh;
    u32x2 a = *(const u32x2*)(qp), bb = *(const u32x2*)(qp + 4);
    u32x2 c = *(const u32x2*)(qp + 16);
    u32x2 d = {0u, 0u};
    if (hh == 0) d = *(const u32x2*)(qp + 20);
    u32x4 x0 = {a[0], a[1], bb[0], bb[1]}, x1 = {c[0], c[1], d[0], d[1]};
    q0[b] = __builtin_bit_cast(bf16x8, x0);
    q1[b] = __builtin_bit_cast(bf16x8, x1);
  }
  auto load_bimg = [&](int pos) {
    const char* qkvb = ws + WS_QKVB;
#pragma unroll
    for (int pc = 0; pc < 18; ++pc) {
      int off = pc * 1024 + lane * 16;
      const char* src;
      if (pc == 0) {
        src = qkvb + (size_t)pos * 2688 + (off < 896 ? off : 0);
      } else {
        int o2 = off - 1024;
        int j = o2 / BSL, col = o2 - j * BSL;
        if (j > 7) j = 7;
        if (col >= 1792) col = 0;
        int pr = pos - 2 * j; if (pr < 0) pr = 0;
        src = qkvb + (size_t)pr * 2688 + 896 + col;
      }
      glds16(src, smem + w * BWB + pc * 1024);
    }
  };

  if (w < 4) load_bimg(p0 + w);
  __builtin_amdgcn_s_waitcnt(0x0F70);
  f32x16 O[4], C[4];
  float R[4];
#pragma unroll
  for (int b = 0; b < 4; ++b) {
    R[b] = 0.f;
#pragma unroll
    for (int r = 0; r < 16; ++r) { O[b][r] = 0.f; C[b][r] = 0.f; }
  }
  {
    bf16x8 nk0 = *(const bf16x8*)(kvp + 4096), nk1 = *(const bf16x8*)(kvp + 4096 + 1024);
    bf16x8 nv0 = *(const bf16x8*)(kvp + 4096 + 2048), nv1 = *(const bf16x8*)(kvp + 4096 + 3072);
#pragma unroll
    for (int b = 0; b < 4; ++b) careful_tile(O[b], C[b], R[b], true, k0, k1, v0, v1, q0[b], q1[b]);
    k0 = nk0; k1 = nk1; v0 = nv0; v1 = nv1;
  }
  asm volatile("" : "+v"(k0), "+v"(k1), "+v"(v0), "+v"(v1));
  for (int t = 1; t < 8; ++t) {
    const int tn = t < 7 ? t + 1 : 7;
    bf16x8 nk0, nk1, nv0, nv1;
    {
      const char* np = kvp + tn * 4096;
      asm volatile("global_load_dwordx4 %0, %4, off\n\tglobal_load_dwordx4 %1, %4, off offset:1024\n\t"
                   "global_load_dwordx4 %2, %4, off offset:2048\n\tglobal_load_dwordx4 %3, %4, off offset:3072"
                   : "=&v"(nk0), "=&v"(nk1), "=&v"(nv0), "=&v"(nv1) : "v"(np) : "memory");
    }
    __builtin_amdgcn_sched_barrier(0);
#pragma unroll
    for (int bp = 0; bp < 2; ++bp) {
      f32x16 sa = __builtin_amdgcn_mfma_f32_32x32x16_bf16(k0, q0[2 * bp], C[2 * bp], 0, 0, 0);
      f32x16 sb = __builtin_amdgcn_mfma_f32_32x32x16_bf16(k0, q0[2 * bp + 1], C[2 * bp + 1], 0, 0, 0);
      sa = __builtin_amdgcn_mfma_f32_32x32x16_bf16(k1, q1[2 * bp], sa, 0, 0, 0);
      sb = __builtin_amdgcn_mfma_f32_32x32x16_bf16(k1, q1[2 * bp + 1], sb, 0, 0, 0);
      pv_tile(O[2 * bp], sa, v0, v1);
      pv_tile(O[2 * bp + 1], sb, v0, v1);
      __builtin_amdgcn_sched_barrier(0);
    }
    asm volatile("s_waitcnt vmcnt(0)" : "+v"(nk0), "+v"(nk1), "+v"(nv0), "+v"(nv1) :: "memory");
    k0 = nk0; k1 = nk1; v0 = nv0; v1 = nv1;
  }
  {
    float lm = fmaxf(fmaxf(O[0][12], O[1][12]), fmaxf(O[2][12], O[3][12]));
    float z = 0.f;
#pragma unroll
    for (int b = 0; b < 4; ++b)
#pragma unroll
      for (int r = 0; r < 16; ++r) z = fmaf(O[b][r], 0.f, z);
    bool bad = ((hh == 1) && !(lm <= PTHR)) || !(z == 0.f);
    if (__builtin_expect(__any(bad), 0)) {
#pragma unroll
      for (int b = 0; b < 4; ++b) {
        R[b] = 0.f;
#pragma unroll
        for (int r = 0; r < 16; ++r) { O[b][r] = 0.f; C[b][r] = 0.f; }
      }
#pragma unroll 1
      for (int t = 0; t < 8; ++t) {
        bf16x8 a0 = *(const bf16x8*)(kvp + t * 4096), a1 = *(const bf16x8*)(kvp + t * 4096 + 1024);
        bf16x8 b0 = *(const bf16x8*)(kvp + t * 4096 + 2048), b1 = *(const bf16x8*)(kvp + t * 4096 + 3072);
#pragma unroll
        for (int b = 0; b < 4; ++b) careful_tile(O[b], C[b], R[b], t == 0, a0, a1, b0, b1, q0[b], q1[b]);
      }
    }
  }

  if (w < 4) {
    WAIT_VM(0);
    local_attn_item(smem + w * BWB, (u16*)(ws + WS_OB), p0 + w, lane >> 3, lane & 7, lane);
    local_attn_item(smem + w * BWB, (u16*)(ws + WS_OB), p0 + w, 8 + (lane >> 3), lane & 7, lane);
    asm volatile("s_waitcnt lgkmcnt(0)" ::: "memory");
    load_bimg(p0 + w + 4);
    WAIT_VM(0);
    local_attn_item(smem + w * BWB, (u16*)(ws + WS_OB), p0 + w + 4, lane >> 3, lane & 7, lane);
    local_attn_item(smem + w * BWB, (u16*)(ws + WS_OB), p0 + w + 4, 8 + (lane >> 3), lane & 7, lane);
    asm volatile("s_waitcnt lgkmcnt(0)" ::: "memory");
  }

  {
    typedef float f32x4 __attribute__((ext_vector_type(4)));
    f32x4* xo = (f32x4*)(smem + w * BWB);
    float* xr = (float*)(smem + w * BWB + 16384);
#pragma unroll
    for (int b = 0; b < 4; ++b) {
#pragma unroll
      for (int g = 0; g < 4; ++g)
        xo[(b * 4 + g) * 64 + lane] = (f32x4){O[b][4 * g], O[b][4 * g + 1], O[b][4 * g + 2], O[b][4 * g + 3]};
      xr[b * 64 + lane] = R[b];
    }
  }
  raw_barrier();
  {
    typedef float f32x4 __attribute__((ext_vector_type(4)));
    const int b = w >> 1;
    float Rs[8], a[8];
#pragma unroll
    for (int i = 0; i < 8; ++i) Rs[i] = ((const float*)(smem + i * BWB + 16384))[b * 64 + lane];
    float Rn = Rs[0];
#pragma unroll
    for (int i = 1; i < 8; ++i) Rn = fmaxf(Rn, Rs[i]);
    float l = 0.f;
#pragma unroll
    for (int i = 0; i < 8; ++i) {
      a[i] = __builtin_amdgcn_exp2f(Rs[i] - Rn);
      l = fmaf(((const f32x4*)(smem + i * BWB))[(b * 4 + 3) * 64 + (lane & 31) + 32][0], a[i], l);
    }
    const float inv = 1.0f / l;
    const int pos = p0 + b * 2 + ((lane >> 4) & 1);
#pragma unroll
    for (int gg = 0; gg < 2; ++gg) {
      const int g = (w & 1) * 2 + gg;
      f32x4 o = {0.f, 0.f, 0.f, 0.f};
#pragma unroll
      for (int i = 0; i < 8; ++i) {
        f32x4 x = ((const f32x4*)(smem + i * BWB))[(b * 4 + g) * 64 + lane];
        o[0] = fmaf(x[0], a[i], o[0]); o[1] = fmaf(x[1], a[i], o[1]);
        o[2] = fmaf(x[2], a[i], o[2]); o[3] = fmaf(x[3], a[i], o[3]);
      }
      if (g < 3 || hh == 0) {
        u32x2 v = {pack_bf16(o[0] * inv, o[1] * inv), pack_bf16(o[2] * inv, o[3] * inv)};
        *(u32x2*)((u16*)(ws + WS_OA) + (size_t)pos * D_ + head * DH + 4 * hh + 8 * g) = v;
      }
    }
  }
}

extern "C" void kernel_launch(void* const* d_in, const int* in_sizes, int n_in,
                              void* d_out, int out_size, void* d_ws, size_t ws_size,
                              hipStream_t stream) {
  const u16* xA = (const u16*)d_in[0];
  const u16* wq = (const u16*)d_in[1];
  const u16* wk = (const u16*)d_in[2];
  const u16* wv = (const u16*)d_in[3];
  const u16* woA = (const u16*)d_in[4];
  const u16* xB = (const u16*)d_in[5];
  const u16* wqkv = (const u16*)d_in[6];
  const u16* woB = (const u16*)d_in[7];
  char* ws = (char*)d_ws;
  const int g_mask = 0xff;
  if (g_mask & 1) proj_kernel<<<256, 64 * GNW, 0, stream>>>(xA, wq, wk, wv, xB, wqkv, ws);
  if (g_mask & 2) attn_kernel<<<256, 512, 0, stream>>>(ws);
  if (g_mask & 4) outproj_kernel<<<224, 64 * GNW, 0, stream>>>(ws, woA, woB, (u16*)d_out);
}
```
